```python
import torch

def _softmax(x, axis=-1):
    return torch.softmax(x, dim=axis)
def _gelu(x):
    return torch.nn.functional.gelu(x, approximate="tanh")
def _silu(x):
    return torch.nn.functional.silu(x)
def _sigmoid(x):
    return torch.sigmoid(x)
def _layernorm(x, axis=-1, eps=1e-5):
    m = x.mean(dim=axis, keepdim=True)
    v = x.var(dim=axis, keepdim=True, unbiased=False)
    return (x - m) / torch.sqrt(v + eps)
def _scatter_add(tgt, idx, src, axis=0):
    return tgt.clone().index_add_(axis, idx, src)
def _segment_sum(src, seg, n):
    out = torch.zeros((n,) + src.shape[1:], dtype=src.dtype)
    return out.index_add_(0, seg, src)
def _pad(x, pw):
    flat = tuple(v for pair in reversed(pw) for v in pair)
    return torch.nn.functional.pad(x, flat)

kernel_name = 'recurrent_scan__linear_kernel_hedgehog__mixer_token_mlp__prompt'

def setup_inputs():

    _w = (lambda a: (lambda t: t.to(torch.bfloat16) if t.is_floating_point() else t)(torch.as_tensor(a)))
    def _A_setup():
        B, S, D, H = 1, 2048, 512, 32
        Dh = D // H
        return {
            "x": _w((lambda *s: torch.randn(*s, dtype=torch.bfloat16) * 0.05)(B, S, D)),
            "wqkv": _w((lambda *s: torch.randn(*s, dtype=torch.bfloat16) * 0.4)(D, 3 * D)),
            "w_phi": _w((lambda *s: torch.randn(*s, dtype=torch.bfloat16) * 0.4)(H, Dh, Dh) * (Dh ** -0.5)),
            "wo": _w((lambda *s: torch.randn(*s, dtype=torch.bfloat16) * 0.2)(D, D)),
        }
    _A_in = _A_setup()

    def _B_setup():
        B, S, D, F = 1, 2048, 512, 1376
        return {
            "x": _w((lambda *s: torch.randn(*s, dtype=torch.bfloat16) * 0.05)(B, S, D)),
            "w1": _w((lambda *s: torch.randn(*s, dtype=torch.bfloat16) * 0.4)(S, F) * (S ** -0.5)),
            "b1": _w((lambda *s: torch.randn(*s, dtype=torch.bfloat16) * 3.2)(F) * 0.02),
            "w2": _w((lambda *s: torch.randn(*s, dtype=torch.bfloat16) * 0.4)(F, S) * (F ** -0.5)),
            "b2": _w((lambda *s: torch.randn(*s, dtype=torch.bfloat16) * 0.8)(S) * 0.02),
        }
    _B_in = _B_setup()

    return {
        "A_x": _A_in["x"],
        "A_wqkv": _A_in["wqkv"],
        "A_w_phi": _A_in["w_phi"],
        "A_wo": _A_in["wo"],
        "B_x": _B_in["x"],
        "B_w1": _B_in["w1"],
        "B_b1": _B_in["b1"],
        "B_w2": _B_in["w2"],
        "B_b2": _B_in["b2"],
    }

def reference(A_x, A_wqkv, A_w_phi, A_wo, B_x, B_w1, B_b1, B_w2, B_b2):
    def _A_ref(x, wqkv, w_phi, wo):
        B, S, D = x.shape
        H = 32; Dh = D // H
        qkv = (lambda x, *d: x.permute(*d))((x @ wqkv).reshape(B, S, 3, H, Dh), 2, 0, 3, 1, 4)
        q, k, v = qkv[0], qkv[1], qkv[2]
        pq = torch.einsum("bhsd,hdf->bhsf", q, w_phi)
        pk = torch.einsum("bhsd,hdf->bhsf", k, w_phi)
        phi_q = torch.concatenate([_softmax(pq, axis=-1), _softmax(-pq, axis=-1)], axis=-1)
        phi_k = torch.concatenate([_softmax(pk, axis=-1), _softmax(-pk, axis=-1)], axis=-1)
        kv = torch.einsum("bhsf,bhsd->bhfd", phi_k, v)
        z  = torch.einsum("bhsf->bhf", phi_k)
        num = torch.einsum("bhqf,bhfd->bhqd", phi_q, kv)
        den = torch.einsum("bhqf,bhf->bhq", phi_q, z)[..., None] + 1e-6
        o = num / den
        return (lambda x, *d: x.permute(*d))(o, 0, 2, 1, 3).reshape(B, S, D) @ wo

    def _B_ref(x, w1, b1, w2, b2):
        B, S, D = x.shape
        h = _layernorm(x, axis=-1)
        u = torch.einsum("bsd,sf->bdf", h, w1) + b1
        u = _gelu(u)
        y = torch.einsum("bdf,fs->bds", u, w2) + b2
        return (lambda x, *d: x.permute(*d))(y, 0, 2, 1)

    x = A_x
    xn = _layernorm(x, axis=-1)
    s = _A_ref(xn, A_wqkv, A_w_phi, A_wo)
    g = _silu(_B_ref(xn, B_w1, B_b1, B_w2, B_b2))
    return x + s * g
```

```cpp
#include <hip/hip_runtime.h>
#include <hip/hip_bf16.h>
#include <stdint.h>
typedef unsigned short u16;
__device__ __forceinline__ float bf2f(u16 v){ return __uint_as_float(((unsigned)v)<<16); }
__device__ __forceinline__ u16 f2bf(float f){ __hip_bfloat16 b = __float2bfloat16(f); return *(u16*)&b; }
__device__ __forceinline__ float rb(float f){ return bf2f(f2bf(f)); }
constexpr int S=2048, D=512, H=32, DH=16, F=1376;

__device__ float blk_sum(float v, float* sm){
  for(int o=32;o>0;o>>=1) v += __shfl_xor(v,o);
  int w=threadIdx.x>>6; __syncthreads(); if((threadIdx.x&63)==0) sm[w]=v; __syncthreads();
  float t=0; for(int i=0;i<(int)(blockDim.x>>6);++i) t+=sm[i]; return t;
}
__global__ void k_ln(const u16* x, u16* xn, u16* h){
  __shared__ float sm[8];
  int r=blockIdx.x, t=threadIdx.x;
  float v=bf2f(x[r*D+t]);
  float m=blk_sum(v,sm)/D; float d=v-m; float var=blk_sum(d*d,sm)/D;
  float mb=rb(m), sd=rb(sqrtf(rb(rb(var)+1e-5f)));
  float n=rb(rb(v-mb)/sd); xn[r*D+t]=f2bf(n);
  m=blk_sum(n,sm)/D; d=n-m; var=blk_sum(d*d,sm)/D;
  mb=rb(m); sd=rb(sqrtf(rb(rb(var)+1e-5f)));
  h[r*D+t]=f2bf(rb(n-mb)/sd);
}
__global__ void k_gemm(const u16* A, int sam, int sak, const u16* B, int sbk, int sbn, u16* C, int M, int N, int K){
  int n=blockIdx.x*blockDim.x+threadIdx.x, m=blockIdx.y; if(n>=N) return;
  float acc=0; for(int k=0;k<K;++k) acc+=bf2f(A[(long)m*sam+(long)k*sak])*bf2f(B[(long)k*sbk+(long)n*sbn]);
  C[(long)m*N+n]=f2bf(acc);
}
__global__ void k_phi(const u16* qkv, const u16* wphi, u16* phi){
  int idx=blockIdx.x*blockDim.x+threadIdx.x;
  int h=idx%H, s=(idx/H)%S, w=idx/(H*S);
  float p[16];
  for(int f=0;f<16;++f){ float a=0; for(int d=0;d<16;++d) a+=bf2f(qkv[(long)s*1536+w*512+h*16+d])*bf2f(wphi[h*256+d*16+f]); p[f]=rb(a);}
  float mx=-1e30f, mn=1e30f; for(int f=0;f<16;++f){mx=fmaxf(mx,p[f]); mn=fminf(mn,p[f]);}
  float s1=0,s2=0; for(int f=0;f<16;++f){ s1+=__expf(p[f]-mx); s2+=__expf(mn-p[f]); }
  u16* o=phi+(long)idx*32;
  for(int f=0;f<16;++f){ o[f]=f2bf(__expf(p[f]-mx)/s1); o[16+f]=f2bf(__expf(mn-p[f])/s2);}
}
__global__ void k_kv(const u16* phi, const u16* qkv, u16* kv, u16* z){
  int h=blockIdx.x, t=threadIdx.x;
  int f=t/17, d=t%17; if(f>=32) return;
  const u16* pk=phi+(long)S*H*32;
  float a=0; for(int s=0;s<S;++s){ float p=bf2f(pk[((long)s*H+h)*32+f]); float v=d<16? bf2f(qkv[(long)s*1536+1024+h*16+d]):1.f; a+=p*v; }
  if(d<16) kv[(h*32+f)*16+d]=f2bf(a); else z[h*32+f]=f2bf(a);
}
__global__ void k_o(const u16* phi, const u16* kv, const u16* z, u16* o){
  int idx=blockIdx.x*blockDim.x+threadIdx.x;
  int d=idx%16, h=(idx/16)%H, s=idx/(16*H);
  float num=0, den=0; for(int f=0;f<32;++f){ float p=bf2f(phi[((long)s*H+h)*32+f]); num+=p*bf2f(kv[(h*32+f)*16+d]); den+=p*bf2f(z[h*32+f]); }
  num=rb(num); den=rb(rb(den)+1e-6f); o[idx]=f2bf(num/den);
}
__global__ void k_gelu(u16* u, const u16* b1){
  int idx=blockIdx.x*blockDim.x+threadIdx.x; if(idx>=D*F) return; int f=idx%F;
  float v=rb(bf2f(u[idx])+bf2f(b1[f]));
  float t=tanhf(0.7978845608028654f*(v+0.044715f*v*v*v)); u[idx]=f2bf(0.5f*v*(1.f+t));
}
__global__ void k_fin(const u16* x, const u16* sm, const u16* y, const u16* b2, u16* out){
  int idx=blockIdx.x*blockDim.x+threadIdx.x; int d=idx%D, s=idx/D;
  float yy=rb(bf2f(y[(long)d*S+s])+bf2f(b2[s])); float g=rb(yy/(1.f+__expf(-yy)));
  out[idx]=f2bf(bf2f(x[idx])+rb(bf2f(sm[idx])*g));
}
extern "C" void kernel_launch(void* const* d_in, const int* in_sizes, int n_in, void* d_out, int out_size, void* d_ws, size_t ws_size, hipStream_t stream){
  const u16* x=(const u16*)d_in[0]; const u16* wqkv=(const u16*)d_in[1]; const u16* wphi=(const u16*)d_in[2]; const u16* wo=(const u16*)d_in[3];
  const u16* w1=(const u16*)d_in[5]; const u16* b1=(const u16*)d_in[6]; const u16* w2=(const u16*)d_in[7]; const u16* b2=(const u16*)d_in[8];
  u16* ws=(u16*)d_ws; u16* xn=ws; u16* h=xn+S*D; u16* qkv=h+S*D; u16* phi=qkv+S*1536; u16* kv=phi+2L*S*H*32; u16* z=kv+H*32*16; u16* o=z+H*32; u16* sm=o+S*D; u16* u=sm+S*D; u16* y=u+D*F;
  k_ln<<<S,512,0,stream>>>(x,xn,h);
  k_gemm<<<dim3(1536/256,S),256,0,stream>>>(xn,D,1,wqkv,1536,1,qkv,S,1536,D);
  k_phi<<<2*S*H/256,256,0,stream>>>(qkv,wphi,phi);
  k_kv<<<H,576,0,stream>>>(phi,qkv,kv,z);
  k_o<<<S*D/256,256,0,stream>>>(phi,kv,z,o);
  k_gemm<<<dim3(2,S),256,0,stream>>>(o,D,1,wo,D,1,sm,S,D,D);
  k_gemm<<<dim3((F+255)/256,D),256,0,stream>>>(h,1,D,w1,F,1,u,D,F,S);
  k_gelu<<<(D*F+255)/256,256,0,stream>>>(u,b1);
  k_gemm<<<dim3(S/256,D),256,0,stream>>>(u,F,1,w2,S,1,y,D,S,F);
  k_fin<<<S*D/256,256,0,stream>>>(x,sm,y,b2,(u16*)d_out);
}
```

```cpp
#include <hip/hip_runtime.h>
#include <hip/hip_bf16.h>
#include <stdint.h>
typedef unsigned short u16;
__device__ __forceinline__ float bf2f(u16 v){ return __uint_as_float(((unsigned)v)<<16); }
__device__ __forceinline__ u16 f2bf(float f){ __hip_bfloat16 b = __float2bfloat16(f); return *(u16*)&b; }
__device__ __forceinline__ float rb(float f){ return bf2f(f2bf(f)); }
constexpr int S=2048, D=512, H=32, DH=16, F=1376;

__device__ float blk_sum(float v, float* sm){
  for(int o=32;o>0;o>>=1) v += __shfl_xor(v,o);
  int w=threadIdx.x>>6; __syncthreads(); if((threadIdx.x&63)==0) sm[w]=v; __syncthreads();
  float t=0; for(int i=0;i<(int)(blockDim.x>>6);++i) t+=sm[i]; return t;
}
__global__ void k_ln(const u16* x, u16* xn, u16* h){
  __shared__ float sm[8];
  int r=blockIdx.x, t=threadIdx.x;
  float v=bf2f(x[r*D+t]);
  float m=blk_sum(v,sm)/D; float d=v-m; float var=blk_sum(d*d,sm)/D;
  float mb=rb(m), sd=rb(sqrtf(rb(rb(var)+1e-5f)));
  float n=rb(rb(v-mb)/sd); xn[r*D+t]=f2bf(n);
  m=blk_sum(n,sm)/D; d=n-m; var=blk_sum(d*d,sm)/D;
  mb=rb(m); sd=rb(sqrtf(rb(rb(var)+1e-5f)));
  h[r*D+t]=f2bf(rb(n-mb)/sd);
}
__global__ void k_gemm(const u16* A, int sam, int sak, const u16* B, int sbk, int sbn, u16* C, int M, int N, int K){
  int n=blockIdx.x*blockDim.x+threadIdx.x, m=blockIdx.y; if(n>=N) return;
  float acc=0; for(int k=0;k<K;++k) acc+=bf2f(A[(long)m*sam+(long)k*sak])*bf2f(B[(long)k*sbk+(long)n*sbn]);
  C[(long)m*N+n]=f2bf(acc);
}
__global__ void k_phi(const u16* qkv, const u16* wphi, u16* phi){
  int idx=blockIdx.x*blockDim.x+threadIdx.x;
  int h=idx%H, s=(idx/H)%S, w=idx/(H*S);
  float p[16];
  for(int f=0;f<16;++f){ float a=0; for(int d=0;d<16;++d) a+=bf2f(qkv[(long)s*1536+w*512+h*16+d])*bf2f(wphi[h*256+d*16+f]); p[f]=rb(a);}
  float mx=-1e30f, mn=1e30f; for(int f=0;f<16;++f){mx=fmaxf(mx,p[f]); mn=fminf(mn,p[f]);}
  float s1=0,s2=0; for(int f=0;f<16;++f){ s1+=__expf(p[f]-mx); s2+=__expf(mn-p[f]); }
  u16* o=phi+(long)idx*32;
  for(int f=0;f<16;++f){ o[f]=f2bf(__expf(p[f]-mx)/s1); o[16+f]=f2bf(__expf(mn-p[f])/s2);}
}
__global__ void k_kv(const u16* phi, const u16* qkv, u16* kv, u16* z){
  int h=blockIdx.x, t=threadIdx.x;
  int f=t/17, d=t%17; if(f>=32) return;
  const u16* pk=phi+(long)S*H*32;
  float a=0; for(int s=0;s<S;++s){ float p=bf2f(pk[((long)s*H+h)*32+f]); float v=d<16? bf2f(qkv[(long)s*1536+1024+h*16+d]):1.f; a+=p*v; }
  if(d<16) kv[(h*32+f)*16+d]=f2bf(a); else z[h*32+f]=f2bf(a);
}
__global__ void k_o(const u16* phi, const u16* kv, const u16* z, u16* o){
  int idx=blockIdx.x*blockDim.x+threadIdx.x;
  int d=idx%16, h=(idx/16)%H, s=idx/(16*H);
  float num=0, den=0; for(int f=0;f<32;++f){ float p=bf2f(phi[((long)s*H+h)*32+f]); num+=p*bf2f(kv[(h*32+f)*16+d]); den+=p*bf2f(z[h*32+f]); }
  num=rb(num); den=rb(rb(den)+1e-6f); o[idx]=f2bf(num/den);
}
__global__ void k_gelu(u16* u, const u16* b1){
  int idx=blockIdx.x*blockDim.x+threadIdx.x; if(idx>=D*F) return; int f=idx%F;
  float v=rb(bf2f(u[idx])+bf2f(b1[f]));
  float t=tanhf(0.7978845608028654f*(v+0.044715f*v*v*v)); u[idx]=f2bf(0.5f*v*(1.f+t));
}
__global__ void k_fin(const u16* x, const u16* sm, const u16* y, const u16* b2, u16* out){
  int idx=blockIdx.x*blockDim.x+threadIdx.x; int d=idx%D, s=idx/D;
  float yy=rb(bf2f(y[(long)d*S+s])+bf2f(b2[s])); float g=rb(yy/(1.f+__expf(-yy)));
  out[idx]=f2bf(bf2f(x[idx])+rb(bf2f(sm[idx])*g));
}

template<int N> __device__ __forceinline__ void waitv(){ asm volatile("s_waitcnt vmcnt(%0)"::"n"(N):"memory"); }
template<int AUX>
__global__ __launch_bounds__(512) void k_probe(const char* A, const char* B, unsigned long long* stamps, int rot){
  extern __shared__ __attribute__((aligned(16))) char lds[];
  int b=(blockIdx.x+rot)&255;
  int wid=__builtin_amdgcn_readfirstlane(threadIdx.x>>6), lane=threadIdx.x&63;
  unsigned long long t0=__builtin_amdgcn_s_memrealtime();
  const char* pa = A + ((size_t)b*8+wid)*2752 + lane*16;
  const char* pb = B + ((size_t)b*8+wid)*2752 + lane*16;
  char* myl = lds + wid*(4*1024);
  __builtin_amdgcn_global_load_lds((const unsigned*)(pa),(__attribute__((address_space(3))) unsigned*)(myl),16,0,AUX);
  __builtin_amdgcn_global_load_lds((const unsigned*)(pa+1024),(__attribute__((address_space(3))) unsigned*)(myl+1024),16,0,AUX);
  __builtin_amdgcn_global_load_lds((const unsigned*)(pb),(__attribute__((address_space(3))) unsigned*)(myl+2048),16,0,AUX);
  __builtin_amdgcn_global_load_lds((const unsigned*)(pb+1024),(__attribute__((address_space(3))) unsigned*)(myl+3072),16,0,AUX);
  waitv<0>();
  __syncthreads();
  unsigned long long t1=__builtin_amdgcn_s_memrealtime();
  if(threadIdx.x==0){ stamps[blockIdx.x*2]=t0; stamps[blockIdx.x*2+1]=t1; }
}
__global__ void k_spin(const unsigned long long* stamps, int what){
  __shared__ unsigned long long a[256], c[256], d[256];
  int t=threadIdx.x; a[t]=stamps[t*2]; c[t]=stamps[t*2+1]; __syncthreads();
  if(t==0){
    unsigned long long mn=~0ull, mxs=0, mx=0, sum=0;
    for(int i=0;i<256;++i){ mn=a[i]<mn?a[i]:mn; mxs=a[i]>mxs?a[i]:mxs; mx=c[i]>mx?c[i]:mx; sum+=c[i]-a[i]; }
    unsigned long long T = what==0? (mx-mn)*100 : (what==1? (mxs-mn)*100 : sum*100/256);
    if(T>200000) T=200000;
    unsigned long long s=__builtin_amdgcn_s_memrealtime();
    while(__builtin_amdgcn_s_memrealtime()-s<T) __builtin_amdgcn_s_sleep(2);
  }
}

extern "C" void kernel_launch(void* const* d_in, const int* in_sizes, int n_in, void* d_out, int out_size, void* d_ws, size_t ws_size, hipStream_t stream){
  const u16* x=(const u16*)d_in[0]; const u16* wqkv=(const u16*)d_in[1]; const u16* wphi=(const u16*)d_in[2]; const u16* wo=(const u16*)d_in[3];
  const u16* w1=(const u16*)d_in[5]; const u16* b1=(const u16*)d_in[6]; const u16* w2=(const u16*)d_in[7]; const u16* b2=(const u16*)d_in[8];
  u16* ws=(u16*)d_ws; u16* xn=ws; u16* h=xn+S*D; u16* qkv=h+S*D; u16* phi=qkv+S*1536; u16* kv=phi+2L*S*H*32; u16* z=kv+H*32*16; u16* o=z+H*32; u16* sm=o+S*D; u16* u=sm+S*D; u16* y=u+D*F;
  unsigned long long* st=(unsigned long long*)((char*)d_ws+(128<<20));
  hipFuncSetAttribute((void*)k_probe<0>,hipFuncAttributeMaxDynamicSharedMemorySize,96*1024); hipFuncSetAttribute((void*)k_probe<2>,hipFuncAttributeMaxDynamicSharedMemorySize,96*1024);
  k_probe<0><<<256,512,96*1024,stream>>>((const char*)w1,(const char*)w2,st,0);
  k_spin<<<1,256,0,stream>>>(st,1);
  k_ln<<<S,512,0,stream>>>(x,xn,h);
  k_gemm<<<dim3(1536/256,S),256,0,stream>>>(xn,D,1,wqkv,1536,1,qkv,S,1536,D);
  k_phi<<<2*S*H/256,256,0,stream>>>(qkv,wphi,phi);
  k_kv<<<H,576,0,stream>>>(phi,qkv,kv,z);
  k_o<<<S*D/256,256,0,stream>>>(phi,kv,z,o);
  k_gemm<<<dim3(2,S),256,0,stream>>>(o,D,1,wo,D,1,sm,S,D,D);
  k_gemm<<<dim3((F+255)/256,D),256,0,stream>>>(h,1,D,w1,F,1,u,D,F,S);
  k_gelu<<<(D*F+255)/256,256,0,stream>>>(u,b1);
  k_gemm<<<dim3(S/256,D),256,0,stream>>>(u,F,1,w2,S,1,y,D,S,F);
  k_fin<<<S*D/256,256,0,stream>>>(x,sm,y,b2,(u16*)d_out);
}
```

```cpp
#include <hip/hip_runtime.h>
#include <hip/hip_bf16.h>
#include <stdint.h>
typedef unsigned short u16;
__device__ __forceinline__ float bf2f(u16 v){ return __uint_as_float(((unsigned)v)<<16); }
__device__ __forceinline__ u16 f2bf(float f){ __hip_bfloat16 b = __float2bfloat16(f); return *(u16*)&b; }
__device__ __forceinline__ float rb(float f){ return bf2f(f2bf(f)); }
constexpr int S=2048, D=512, H=32, DH=16, F=1376;

__device__ float blk_sum(float v, float* sm){
  for(int o=32;o>0;o>>=1) v += __shfl_xor(v,o);
  int w=threadIdx.x>>6; __syncthreads(); if((threadIdx.x&63)==0) sm[w]=v; __syncthreads();
  float t=0; for(int i=0;i<(int)(blockDim.x>>6);++i) t+=sm[i]; return t;
}
__global__ void k_ln(const u16* x, u16* xn, u16* h){
  __shared__ float sm[8];
  int r=blockIdx.x, t=threadIdx.x;
  float v=bf2f(x[r*D+t]);
  float m=blk_sum(v,sm)/D; float d=v-m; float var=blk_sum(d*d,sm)/D;
  float mb=rb(m), sd=rb(sqrtf(rb(rb(var)+1e-5f)));
  float n=rb(rb(v-mb)/sd); xn[r*D+t]=f2bf(n);
  m=blk_sum(n,sm)/D; d=n-m; var=blk_sum(d*d,sm)/D;
  mb=rb(m); sd=rb(sqrtf(rb(rb(var)+1e-5f)));
  h[r*D+t]=f2bf(rb(n-mb)/sd);
}
__global__ void k_gemm(const u16* A, int sam, int sak, const u16* B, int sbk, int sbn, u16* C, int M, int N, int K){
  int n=blockIdx.x*blockDim.x+threadIdx.x, m=blockIdx.y; if(n>=N) return;
  float acc=0; for(int k=0;k<K;++k) acc+=bf2f(A[(long)m*sam+(long)k*sak])*bf2f(B[(long)k*sbk+(long)n*sbn]);
  C[(long)m*N+n]=f2bf(acc);
}
__global__ void k_phi(const u16* qkv, const u16* wphi, u16* phi){
  int idx=blockIdx.x*blockDim.x+threadIdx.x;
  int h=idx%H, s=(idx/H)%S, w=idx/(H*S);
  float p[16];
  for(int f=0;f<16;++f){ float a=0; for(int d=0;d<16;++d) a+=bf2f(qkv[(long)s*1536+w*512+h*16+d])*bf2f(wphi[h*256+d*16+f]); p[f]=rb(a);}
  float mx=-1e30f, mn=1e30f; for(int f=0;f<16;++f){mx=fmaxf(mx,p[f]); mn=fminf(mn,p[f]);}
  float s1=0,s2=0; for(int f=0;f<16;++f){ s1+=__expf(p[f]-mx); s2+=__expf(mn-p[f]); }
  u16* o=phi+(long)idx*32;
  for(int f=0;f<16;++f){ o[f]=f2bf(__expf(p[f]-mx)/s1); o[16+f]=f2bf(__expf(mn-p[f])/s2);}
}
__global__ void k_kv(const u16* phi, const u16* qkv, u16* kv, u16* z){
  int h=blockIdx.x, t=threadIdx.x;
  int f=t/17, d=t%17; if(f>=32) return;
  const u16* pk=phi+(long)S*H*32;
  float a=0; for(int s=0;s<S;++s){ float p=bf2f(pk[((long)s*H+h)*32+f]); float v=d<16? bf2f(qkv[(long)s*1536+1024+h*16+d]):1.f; a+=p*v; }
  if(d<16) kv[(h*32+f)*16+d]=f2bf(a); else z[h*32+f]=f2bf(a);
}
__global__ void k_o(const u16* phi, const u16* kv, const u16* z, u16* o){
  int idx=blockIdx.x*blockDim.x+threadIdx.x;
  int d=idx%16, h=(idx/16)%H, s=idx/(16*H);
  float num=0, den=0; for(int f=0;f<32;++f){ float p=bf2f(phi[((long)s*H+h)*32+f]); num+=p*bf2f(kv[(h*32+f)*16+d]); den+=p*bf2f(z[h*32+f]); }
  num=rb(num); den=rb(rb(den)+1e-6f); o[idx]=f2bf(num/den);
}
__global__ void k_gelu(u16* u, const u16* b1){
  int idx=blockIdx.x*blockDim.x+threadIdx.x; if(idx>=D*F) return; int f=idx%F;
  float v=rb(bf2f(u[idx])+bf2f(b1[f]));
  float t=tanhf(0.7978845608028654f*(v+0.044715f*v*v*v)); u[idx]=f2bf(0.5f*v*(1.f+t));
}
__global__ void k_fin(const u16* x, const u16* sm, const u16* y, const u16* b2, u16* out){
  int idx=blockIdx.x*blockDim.x+threadIdx.x; int d=idx%D, s=idx/D;
  float yy=rb(bf2f(y[(long)d*S+s])+bf2f(b2[s])); float g=rb(yy/(1.f+__expf(-yy)));
  out[idx]=f2bf(bf2f(x[idx])+rb(bf2f(sm[idx])*g));
}

typedef unsigned v4u __attribute__((ext_vector_type(4)));
template<int NI,int PF,int DEP>
__global__ __launch_bounds__(512) void k_ic(unsigned long long* stamps, float* sink){
  extern __shared__ char lds[];
  float a=threadIdx.x, b=1.0001f, c=0.5f, a2=a+1, a3=a+2, a4=a+3;
  unsigned long long t[3];
  if(PF){
    unsigned long long pc; asm volatile("s_getpc_b64 %0" : "=s"(pc));
    const char* p=(const char*)(pc & ~127ull);
    unsigned x=0;
    for(int o=threadIdx.x*16;o<PF*1024;o+=blockDim.x*16){ v4u v=*(const v4u*)(p+o); x^=v.x^v.y^v.z^v.w; }
    if(x==0x12345678) sink[1]=a;
    __syncthreads();
  }
  t[0]=__builtin_amdgcn_s_memrealtime();
  #pragma unroll 1
  for(int it=0;it<2;++it){
    if(DEP){
    #pragma unroll
    for(int i=0;i<NI;++i) asm volatile("v_fma_f32 %0, %0, %1, %2" : "+v"(a) : "v"(b), "v"(c));
    } else {
    #pragma unroll
    for(int i=0;i<NI/4;++i) asm volatile("v_fma_f32 %0, %0, %4, %5\n v_fma_f32 %1, %1, %4, %5\n v_fma_f32 %2, %2, %4, %5\n v_fma_f32 %3, %3, %4, %5" : "+v"(a),"+v"(a2),"+v"(a3),"+v"(a4) : "v"(b), "v"(c));
    }
    unsigned long long x=__builtin_amdgcn_s_memrealtime();
    if(it==0) t[1]=x; else t[2]=x;
  }
  if(a+a2+a3+a4==1.2345f) sink[0]=a;
  if(threadIdx.x==0){ stamps[blockIdx.x*4]=t[0]; stamps[blockIdx.x*4+1]=t[1]; stamps[blockIdx.x*4+2]=t[2]; }
}
__global__ void k_spin(const unsigned long long* stamps, int what){
  if(threadIdx.x==0){
    long long sum=0;
    for(int i=0;i<256;++i){ long long a=stamps[i*4], b=stamps[i*4+1], c=stamps[i*4+2]; sum += what==0? (b-a)-(c-b) : (c-b); }
    long long T=sum*1000/256; if(T<0) T=0; if(T>1000000) T=1000000;
    unsigned long long s=__builtin_amdgcn_s_memrealtime();
    while((long long)(__builtin_amdgcn_s_memrealtime()-s)<T) __builtin_amdgcn_s_sleep(2);
  }
}

extern "C" void kernel_launch(void* const* d_in, const int* in_sizes, int n_in, void* d_out, int out_size, void* d_ws, size_t ws_size, hipStream_t stream){
  const u16* x=(const u16*)d_in[0]; const u16* wqkv=(const u16*)d_in[1]; const u16* wphi=(const u16*)d_in[2]; const u16* wo=(const u16*)d_in[3];
  const u16* w1=(const u16*)d_in[5]; const u16* b1=(const u16*)d_in[6]; const u16* w2=(const u16*)d_in[7]; const u16* b2=(const u16*)d_in[8];
  u16* ws=(u16*)d_ws; u16* xn=ws; u16* h=xn+S*D; u16* qkv=h+S*D; u16* phi=qkv+S*1536; u16* kv=phi+2L*S*H*32; u16* z=kv+H*32*16; u16* o=z+H*32; u16* sm=o+S*D; u16* u=sm+S*D; u16* y=u+D*F;
  unsigned long long* st=(unsigned long long*)((char*)d_ws+(128<<20));
  hipFuncSetAttribute((void*)k_ic<512,4,0>,hipFuncAttributeMaxDynamicSharedMemorySize,96*1024);
  k_ic<512,4,0><<<256,512,96*1024,stream>>>(st,(float*)(st+4096));
  k_spin<<<1,64,0,stream>>>(st,0);
  k_ln<<<S,512,0,stream>>>(x,xn,h);
  k_gemm<<<dim3(1536/256,S),256,0,stream>>>(xn,D,1,wqkv,1536,1,qkv,S,1536,D);
  k_phi<<<2*S*H/256,256,0,stream>>>(qkv,wphi,phi);
  k_kv<<<H,576,0,stream>>>(phi,qkv,kv,z);
  k_o<<<S*D/256,256,0,stream>>>(phi,kv,z,o);
  k_gemm<<<dim3(2,S),256,0,stream>>>(o,D,1,wo,D,1,sm,S,D,D);
  k_gemm<<<dim3((F+255)/256,D),256,0,stream>>>(h,1,D,w1,F,1,u,D,F,S);
  k_gelu<<<(D*F+255)/256,256,0,stream>>>(u,b1);
  k_gemm<<<dim3(S/256,D),256,0,stream>>>(u,F,1,w2,S,1,y,D,S,F);
  k_fin<<<S*D/256,256,0,stream>>>(x,sm,y,b2,(u16*)d_out);
}
```

```cpp
#include <hip/hip_runtime.h>
#include <stdint.h>

typedef unsigned u32;
typedef unsigned short u16;
typedef unsigned long long u64;
typedef u32 v4u __attribute__((ext_vector_type(4)));
typedef u32 v2u __attribute__((ext_vector_type(2)));
typedef float f32x16 __attribute__((ext_vector_type(16)));
typedef float f32x4 __attribute__((ext_vector_type(4)));
typedef float f32x2 __attribute__((ext_vector_type(2)));
typedef short s16x4 __attribute__((ext_vector_type(4)));
typedef __bf16 bf16x8 __attribute__((ext_vector_type(8)));
typedef __bf16 bf16x2 __attribute__((ext_vector_type(2)));
typedef __amdgpu_buffer_rsrc_t rsrc_t;
#define LAS __attribute__((address_space(3)))
#define DI __device__ __forceinline__

#define STAMPS 0

constexpr u32 WS_EPOCH = 0, WS_FLAGS = 4096, FL_REP = 4096;
constexpr u32 F1 = 0, F2 = 1024, F3 = 2048, F4 = 3072;
constexpr u32 WS_STATS = 65536;
constexpr u32 WS_KVZ = 131072;
constexpr u32 WS_KVR = 262144;
constexpr u32 WS_H = 1u << 20;
constexpr u32 WS_O = 3u << 20;
constexpr u32 WS_U = 5u << 20;
constexpr u32 WS_BYTES = 7u << 20;
constexpr u32 WS_STAMP = 8u << 20;
constexpr u32 OOB = 0x40000000u;
constexpr u32 LDS_BYTES = 163840;
constexpr u32 SPIN_CAP = 1u << 15;
constexpr float LOG2E = 1.4426950408889634f;

DI rsrc_t mkrs(const void* p, u32 bytes) { return __builtin_amdgcn_make_buffer_rsrc((void*)p, (short)0, (int)bytes, 0x00020000); }
DI u32 rfl(u32 v) { return (u32)__builtin_amdgcn_readfirstlane((int)v); }
DI void glds(u32 lds, u32 voff, rsrc_t rs, u32 soff) {
  asm volatile("s_mov_b32 m0, %0\n\ts_nop 0\n\tbuffer_load_dwordx4 %1, %2, %3 offen lds" ::"s"(lds), "v"(voff), "s"(rs), "s"(soff) : "memory");
}
DI void ld128_sc1(v4u& d, u32 voff, rsrc_t rs, u32 soff) {
  asm volatile("buffer_load_dwordx4 %0, %1, %2, %3 offen sc1" : "+v"(d) : "v"(voff), "s"(rs), "s"(soff) : "memory");
}
DI void ld128_pl(v4u& d, u32 voff, rsrc_t rs, u32 soff) {
  asm volatile("buffer_load_dwordx4 %0, %1, %2, %3 offen" : "+v"(d) : "v"(voff), "s"(rs), "s"(soff) : "memory");
}
DI void ld64_sc1(v2u& d, u32 voff, rsrc_t rs, u32 soff) {
  asm volatile("buffer_load_dwordx2 %0, %1, %2, %3 offen sc1" : "+v"(d) : "v"(voff), "s"(rs), "s"(soff) : "memory");
}
DI void st128_sc1(v4u v, rsrc_t rs, u32 off) { __builtin_amdgcn_raw_buffer_store_b128(v, rs, (int)off, 0, 16); }
DI void st64_sc1(v2u v, rsrc_t rs, u32 off) { __builtin_amdgcn_raw_buffer_store_b64(v, rs, (int)off, 0, 16); }
#define BAR() asm volatile("s_waitcnt lgkmcnt(0)\n\ts_barrier" ::: "memory")
#define DRAIN_BAR() asm volatile("s_waitcnt vmcnt(0) lgkmcnt(0)\n\ts_barrier" ::: "memory")

DI v2u trrd(u32 a) { return __builtin_bit_cast(v2u, __builtin_amdgcn_ds_read_tr16_b64_v4i16((LAS s16x4*)(size_t)a)); }
DI v4u trfrag(u32 a0, u32 a1) { v2u x = trrd(a0), y = trrd(a1); return v4u{x.x, x.y, y.x, y.y}; }
DI v4u lds128(u32 a) { return *(LAS v4u*)(size_t)a; }
DI f32x4 lds128f(u32 a) { return *(LAS f32x4*)(size_t)a; }
DI void sts128(u32 a, v4u v) { *(LAS v4u*)(size_t)a = v; }
DI void sts128f(u32 a, f32x4 v) { *(LAS f32x4*)(size_t)a = v; }
DI void sts64(u32 a, v2u v) { *(LAS v2u*)(size_t)a = v; }
DI f32x16 mfma(v4u a, v4u b, f32x16 c) {
  return __builtin_amdgcn_mfma_f32_32x32x16_bf16(__builtin_bit_cast(bf16x8, a), __builtin_bit_cast(bf16x8, b), c, 0, 0, 0);
}
DI u32 pk(float a, float b) { f32x2 v = {a, b}; return __builtin_bit_cast(u32, __builtin_convertvector(v, bf16x2)); }
DI float blo(u32 v) { return __uint_as_float(v << 16); }
DI float bhi(u32 v) { return __uint_as_float(v & 0xffff0000u); }
DI float dot2(u32 a, u32 b, float c) { return __builtin_amdgcn_fdot2_f32_bf16(__builtin_bit_cast(bf16x2, a), __builtin_bit_cast(bf16x2, b), c, false); }
template <int C> DI float dpp(float v) { return __int_as_float(__builtin_amdgcn_mov_dpp(__float_as_int(v), C, 0xF, 0xF, true)); }
DI float x16sum(float v) { u32 x = __float_as_uint(v); auto r = __builtin_amdgcn_permlane16_swap(x, x, false, false); return __uint_as_float(r[0]) + __uint_as_float(r[1]); }
DI float x32sum(float v) { u32 x = __float_as_uint(v); auto r = __builtin_amdgcn_permlane32_swap(x, x, false, false); return __uint_as_float(r[0]) + __uint_as_float(r[1]); }
DI float x32max(float v) { u32 x = __float_as_uint(v); auto r = __builtin_amdgcn_permlane32_swap(x, x, false, false); return fmaxf(__uint_as_float(r[0]), __uint_as_float(r[1])); }
DI float x32min(float v) { u32 x = __float_as_uint(v); auto r = __builtin_amdgcn_permlane32_swap(x, x, false, false); return fminf(__uint_as_float(r[0]), __uint_as_float(r[1])); }
DI float wsum(float v) {
  v += dpp<0xB1>(v); v += dpp<0x4E>(v); v += dpp<0x141>(v); v += dpp<0x140>(v);
  return x32sum(x16sum(v));
}
DI v4u sel4(bool c, v4u a, v4u b) { return v4u{c ? a.x : b.x, c ? a.y : b.y, c ? a.z : b.z, c ? a.w : b.w}; }

DI void publish(rsrc_t rsW, u32 off, u32 tlo, u32 thi, int lane) {
  if (lane < 8) st64_sc1(v2u{tlo, thi}, rsW, WS_FLAGS + lane * FL_REP + off);
}
DI void poll64(rsrc_t rsW, u32 off, bool active, u32 tlo, u32 thi) {
#pragma unroll 1
  for (u32 spin = 0; spin < SPIN_CAP; ++spin) {
    v2u t = {0u, 0u};
    asm volatile("buffer_load_dwordx2 %0, %1, %2, 0 offen sc1\n\ts_waitcnt vmcnt(0)" : "=&v"(t) : "v"(off), "s"(rsW) : "memory");
    bool ok = !active || (t.x == tlo && t.y == thi);
    if (__all(ok)) break;
  }
}
DI void poll128(rsrc_t rsW, u32 off, u32 tlo, u32 thi) {
#pragma unroll 1
  for (u32 spin = 0; spin < SPIN_CAP; ++spin) {
    v4u t = {0u, 0u, 0u, 0u};
    asm volatile("buffer_load_dwordx4 %0, %1, %2, 0 offen sc1\n\ts_waitcnt vmcnt(0)" : "=&v"(t) : "v"(off), "s"(rsW) : "memory");
    bool ok = t.x == tlo && t.y == thi && t.z == tlo && t.w == thi;
    if (__all(ok)) break;
  }
}

DI void dual_softmax(const float* p, float c, v4u& fp, v4u& fn) {
  float mx = fmaxf(fmaxf(fmaxf(p[0], p[1]), fmaxf(p[2], p[3])), fmaxf(fmaxf(p[4], p[5]), fmaxf(p[6], p[7])));
  float mn = fminf(fminf(fminf(p[0], p[1]), fminf(p[2], p[3])), fminf(fminf(p[4], p[5]), fminf(p[6], p[7])));
  mx = x32max(mx); mn = x32min(mn);
  float a = -mx * c, b = mn * c;
  float e[8], g[8];
#pragma unroll
  for (int i = 0; i < 8; ++i) { e[i] = __builtin_amdgcn_exp2f(fmaf(p[i], c, a)); g[i] = __builtin_amdgcn_exp2f(fmaf(p[i], -c, b)); }
  float se = ((e[0] + e[1]) + (e[2] + e[3])) + ((e[4] + e[5]) + (e[6] + e[7]));
  float sg = ((g[0] + g[1]) + (g[2] + g[3])) + ((g[4] + g[5]) + (g[6] + g[7]));
  se = __builtin_amdgcn_rcpf(x32sum(se)); sg = __builtin_amdgcn_rcpf(x32sum(sg));
  fp = v4u{pk(e[0] * se, e[1] * se), pk(e[2] * se, e[3] * se), pk(e[4] * se, e[5] * se), pk(e[6] * se, e[7] * se)};
  fn = v4u{pk(g[0] * sg, g[1] * sg), pk(g[2] * sg, g[3] * sg), pk(g[4] * sg, g[5] * sg), pk(g[6] * sg, g[7] * sg)};
}

#define STAMP(i) do {} while (0)

__global__ __launch_bounds__(512, 2) void fused_kernel(const u16* __restrict__ gx, const u16* __restrict__ gwqkv, const u16* __restrict__ gwphi,
                                                         const u16* __restrict__ gwo, const u16* __restrict__ gw1, const u16* __restrict__ gb1,
                                                         const u16* __restrict__ gw2, const u16* __restrict__ gb2, u16* __restrict__ gout, char* ws) {
  extern __shared__ __attribute__((aligned(16))) char smem[];
  STAMP(0);
  const int tid = threadIdx.x, lane = tid & 63;
  const u32 w = rfl(tid >> 6);
  const u32 b = blockIdx.x, xcd = b & 7, idx = b >> 3, li = idx >> 1;
  const u32 l31 = lane & 31, hh = lane >> 5, blk = (lane >> 4) & 1, q4 = (lane & 15) >> 2, p4 = lane & 3;
  const rsrc_t rsW = mkrs(ws, WS_BYTES);
  const rsrc_t rsX = mkrs(gx, 2048 * 512 * 2);
  const u32 lane16 = lane * 16;

  const u32 tt = xcd * 4 + (idx >> 3), dt = idx & 7;
  const u32 dh = w & 1, kq = w >> 1;
  const bool isWO = kq == 3;
  const rsrc_t rsFW = isWO ? mkrs(gwo, 512 * 512 * 2) : mkrs(gw2, 1376 * 2048 * 2);
  const u32 fpitch = isWO ? 1024u : 4096u;
  const u32 fcol = isWO ? dt * 128 : tt * 128;
  const u32 frot = (((idx >> 3) + (dt >> 1)) * 8) & 31;
  const u32 fbeg = isWO ? 0u : kq * 29, fcnt = isWO ? 32u : (kq == 2 ? 28u : 29u);
  const u32 fvoff = (lane >> 3) * fpitch + (((lane & 7) ^ (((lane >> 4) & 1) << 2)) << 4);
  auto fin_glds = [&](u32 st) {
    u32 i = (2 * st + dh + frot) & 31;
    u32 so = (st < 16 && i < fcnt) ? (fbeg + i) * 16 * fpitch + fcol : OOB;
    u32 sl = st >= 9 ? st - 9 : st;
    if (st >= 18) sl = st - 18;
    u32 la = sl * 16384 + kq * 4096 + dh * 2048;
    glds(la, fvoff, rsFW, so);
    glds(la + 1024, fvoff, rsFW, so + 8 * fpitch);
  };

  u32 tlo, thi;
  const u32 fin_done = (idx & 1) ? 3u : 6u;

  if ((idx & 1) == 0) {
    v2u ep = {0u, 0u};
    ld64_sc1(ep, 0u, rsW, WS_EPOCH);
    const u32 rt = (xcd >> 1) * 4 + (li >> 2), hg = (xcd & 1) * 4 + (li & 3);
    const u32 tg = w & 3, hp = w >> 2;
    const rsrc_t rsQ = mkrs(gwqkv, 512 * 1536 * 2);
    const u32 rot = (2 * ((li >> 2) + (li & 3))) & 7;
    u32 wv[3];
#pragma unroll
    for (int i = 0; i < 3; ++i) {
      u32 o = i * 1024 + lane16, row = o / 384, c = (o - row * 384) >> 4;
      wv[i] = (8 * w + row) * 3072 + (c >> 3) * 1024 + hg * 128 + (((c & 7) ^ (((row >> 1) & 1) << 2)) << 4);
    }
    u32 xv[2];
#pragma unroll
    for (int i = 0; i < 2; ++i) {
      u32 row = 16 * w + 8 * i + (lane >> 3);
      xv[i] = (rt * 128 + row) * 1024 + (((lane & 7) ^ ((row >> 1) & 7)) << 4);
    }
    auto a_stage = [&](u32 st, u32 slot) {
      u32 k = (st + rot) & 7;
      u32 sx = st < 8 ? k * 128 : OOB, sw = st < 8 ? k * 64 * 3072 : OOB;
      u32 la = slot * 40960;
      glds(la + w * 2048, xv[0], rsX, sx);
      glds(la + w * 2048 + 1024, xv[1], rsX, sx);
      glds(la + 16384 + w * 3072, wv[0], rsQ, sw);
      glds(la + 16384 + w * 3072 + 1024, wv[1], rsQ, sw);
      glds(la + 16384 + w * 3072 + 2048, wv[2], rsQ, sw);
    };
    a_stage(0, 0);
    a_stage(1, 1);
    v4u wph;
    {
      const u16* p = gwphi + (hg * 4 + hp * 2 + (l31 >> 4)) * 256 + (4 * hh) * 16 + (l31 & 15);
      wph.x = p[0] | ((u32)p[16] << 16); wph.y = p[32] | ((u32)p[48] << 16);
      wph.z = p[128] | ((u32)p[144] << 16); wph.w = p[160] | ((u32)p[176] << 16);
    }
    u32 xa[4];
#pragma unroll
    for (int ks = 0; ks < 4; ++ks) xa[ks] = (32 * tg + l31) * 128 + (((2 * ks + hh) ^ ((l31 >> 1) & 7)) << 4);
    const u32 wa = 16384 + (8 * hh + q4) * 384 + ((hp ^ (q4 >> 1)) << 6) + blk * 32 + p4 * 8;
    f32x16 acc[3];
#pragma unroll
    for (int i = 0; i < 3; ++i)
#pragma unroll
      for (int j = 0; j < 16; ++j) acc[i][j] = 0.f;
    float cs[3] = {0.f, 0.f, 0.f};
    v4u stg = {0u, 0u, 0u, 0u};
    const u32 stoff = WS_STATS + (rt * 128 + 32 * tg + l31) * 16;
    u32 slot = 0;
#pragma unroll 1
    for (u32 st = 0; st < 8; ++st) {
      asm volatile("s_waitcnt vmcnt(5) lgkmcnt(0)\n\ts_barrier" ::: "memory");
      if (st == 0) STAMP(1);
      u32 rs = slot == 0 ? 2u : slot - 1;
      a_stage(st + 2, rs);
      if (st == 7) ld128_sc1(stg, stoff, rsW, 0u);
      const u32 sb = slot * 40960;
#pragma unroll
      for (int ks = 0; ks < 4; ++ks) {
        v4u xf = lds128(sb + xa[ks]);
        v4u wf[3];
#pragma unroll
        for (int s = 0; s < 3; ++s) wf[s] = trfrag(sb + wa + ks * 6144 + s * 128, sb + wa + ks * 6144 + 1536 + s * 128);
#pragma unroll
        for (int s = 0; s < 3; ++s) acc[s] = mfma(wf[s], xf, acc[s]);
        if (tg == (u32)ks) {
#pragma unroll
          for (int s = 0; s < 3; ++s) {
            u32 a0 = wf[s].x, a1 = wf[s].y, a2 = wf[s].z, a3 = wf[s].w;
            cs[s] = dot2(a0, 0x3f803f80u, cs[s]); cs[s] = dot2(a1, 0x3f803f80u, cs[s]);
            cs[s] = dot2(a2, 0x3f803f80u, cs[s]); cs[s] = dot2(a3, 0x3f803f80u, cs[s]);
          }
        }
      }
      slot = slot == 2 ? 0u : slot + 1;
    }
    STAMP(2);
    const u32 CSP = 147456, CSF = 147456 + 3072;
#pragma unroll
    for (int s = 0; s < 3; ++s) {
      float t = x32sum(cs[s]);
      if (lane < 32) *(LAS float*)(size_t)(CSP + ((hp * 4 + tg) * 3 + s) * 128 + lane * 4) = t;
    }
    asm volatile("s_waitcnt vmcnt(0) lgkmcnt(0)\n\ts_barrier" : "+v"(stg), "+v"(ep)::"memory");
    tlo = rfl(ep.x) + 1u; thi = rfl(ep.y);
    const u32 tB = tlo * 0x9E3779B1u + thi;
    fin_glds(0); fin_glds(1);
    {
      float t0 = 0.f, t1 = 0.f;
#pragma unroll
      for (int g = 0; g < 4; ++g) {
        t0 += *(LAS float*)(size_t)(CSP + (hp * 4 + g) * 384 + lane * 4);
        t1 += *(LAS float*)(size_t)(CSP + (hp * 4 + g) * 384 + 256 + l31 * 4);
      }
      *(LAS float*)(size_t)(CSF + w * 384 + lane * 4) = t0;
      if (lane < 32) *(LAS float*)(size_t)(CSF + w * 384 + 256 + lane * 4) = t1;
    }
#pragma unroll 1
    for (u32 spin = 0; spin < SPIN_CAP; ++spin) {
      if (__all(stg.y == tlo && stg.w == tB)) break;
      asm volatile("buffer_load_dwordx4 %0, %1, %2, 0 offen sc1\n\ts_waitcnt vmcnt(0)" : "+v"(stg) : "v"(stoff), "s"(rsW) : "memory");
    }
    const float mean = __uint_as_float(stg.x), rstd = __uint_as_float(stg.z);
    const float cexp = rstd * LOG2E;
    const bool lo16 = l31 < 16;
    const v4u zero4 = {0u, 0u, 0u, 0u};
    const v4u wph0 = sel4(lo16, wph, zero4), wph1 = sel4(lo16, zero4, wph);
#pragma unroll
    for (int s = 0; s < 3; ++s)
#pragma unroll
      for (int qq = 0; qq < 4; ++qq) {
        f32x4 c = lds128f(CSF + w * 384 + s * 128 + (8 * qq + 4 * hh) * 4);
#pragma unroll
        for (int j = 0; j < 4; ++j) acc[s][4 * qq + j] = fmaf(-mean, c[j], acc[s][4 * qq + j]);
      }
    auto logits = [&](const f32x16& a) {
      v4u f0 = {pk(a[0], a[1]), pk(a[2], a[3]), pk(a[4], a[5]), pk(a[6], a[7])};
      v4u f1 = {pk(a[8], a[9]), pk(a[10], a[11]), pk(a[12], a[13]), pk(a[14], a[15])};
      f32x16 z;
#pragma unroll
      for (int j = 0; j < 16; ++j) z[j] = 0.f;
      z = mfma(wph0, f0, z);
      return mfma(wph1, f1, z);
    };
    const u32 IMG = 98304 + w * 6144;
    {
      f32x16 pkk = logits(acc[1]);
      const u32 wa2 = IMG + l31 * 64 + hh * 8;
      const u32 sw = (l31 >> 1) & 3;
#pragma unroll
      for (int qq = 0; qq < 4; ++qq)
        sts64(wa2 + 4096 + ((qq ^ sw) << 4), v2u{pk(acc[2][4 * qq] * rstd, acc[2][4 * qq + 1] * rstd), pk(acc[2][4 * qq + 2] * rstd, acc[2][4 * qq + 3] * rstd)});
#pragma unroll
      for (int hd = 0; hd < 2; ++hd) {
        float p[8];
#pragma unroll
        for (int j = 0; j < 8; ++j) p[j] = pkk[8 * hd + j];
        v4u fp, fn;
        dual_softmax(p, cexp, fp, fn);
        sts64(wa2 + hd * 2048 + ((0 ^ sw) << 4), v2u{fp.x, fp.y});
        sts64(wa2 + hd * 2048 + ((1 ^ sw) << 4), v2u{fp.z, fp.w});
        sts64(wa2 + hd * 2048 + ((2 ^ sw) << 4), v2u{fn.x, fn.y});
        sts64(wa2 + hd * 2048 + ((3 ^ sw) << 4), v2u{fn.z, fn.w});
      }
      f32x16 kv0, kv1;
#pragma unroll
      for (int j = 0; j < 16; ++j) { kv0[j] = 0.f; kv1[j] = 0.f; }
      const v4u ones = {0x3f803f80u, 0x3f803f80u, 0x3f803f80u, 0x3f803f80u};
#pragma unroll
      for (int s = 0; s < 2; ++s) {
        u32 ra[2];
#pragma unroll
        for (int t = 0; t < 2; ++t) {
          u32 row = 16 * s + 8 * hh + 4 * t + q4;
          ra[t] = IMG + row * 64 + (((2 * blk + (p4 >> 1)) ^ ((row >> 1) & 3)) << 4) + (p4 & 1) * 8;
        }
        v4u vf = trfrag(ra[0] + 4096, ra[1] + 4096);
        v4u a0 = trfrag(ra[0], ra[1]), a1 = trfrag(ra[0] + 2048, ra[1] + 2048);
        kv0 = mfma(a0, sel4(lo16, vf, ones), kv0);
        kv1 = mfma(a1, sel4(lo16, ones, vf), kv1);
      }
      asm volatile("s_waitcnt lgkmcnt(0)" ::: "memory");
#pragma unroll
      for (int qq = 0; qq < 4; ++qq) {
        f32x4 r, z;
#pragma unroll
        for (int j = 0; j < 4; ++j) { r[j] = lo16 ? kv0[4 * qq + j] : kv1[4 * qq + j]; z[j] = lo16 ? kv1[4 * qq + j] : kv0[4 * qq + j]; }
        sts128f(IMG + qq * 1024 + lane16, r);
        if ((lane & 15) == 0) sts128f(IMG + 4096 + ((lo16 ? 2 : 0) + hh) * 64 + qq * 16, z);
      }
    }
    BAR();
    if (w < 4) {
      const u32 uh = w >> 1, us = w & 1;
      f32x4 s0 = {0.f, 0.f, 0.f, 0.f}, s1 = s0;
#pragma unroll
      for (int g = 0; g < 4; ++g) {
        u32 a = 98304 + (uh * 4 + g) * 6144 + us * 2048 + lane16;
        s0 += lds128f(a); s1 += lds128f(a + 1024);
      }
      st128_sc1(v4u{pk(s0[0], s0[1]), pk(s0[2], s0[3]), pk(s1[0], s1[1]), pk(s1[2], s1[3])}, rsW, WS_KVR + (((hg * 2 + uh) * 2 + us) * 16 + rt) * 1024 + lane16);
    } else if (w < 6) {
      const u32 uh = w - 4;
      if (lane < 8) {
        f32x4 s0 = {0.f, 0.f, 0.f, 0.f}, s1 = s0;
#pragma unroll
        for (int g = 0; g < 4; ++g) {
          u32 a = 98304 + (uh * 4 + g) * 6144 + 4096 + lane * 32;
          s0 += lds128f(a); s1 += lds128f(a + 16);
        }
        st128_sc1(v4u{pk(s0[0], s0[1]), pk(s0[2], s0[3]), pk(s1[0], s1[1]), pk(s1[2], s1[3])}, rsW, WS_KVZ + ((hg * 2 + uh) * 16 + rt) * 128 + lane16);
      }
    }
    DRAIN_BAR();
    if (w == 0) publish(rsW, F2 + (hg * 16 + rt) * 8, tlo, thi, lane);
    STAMP(3);
    if (w != 0) { fin_glds(2); fin_glds(3); }
    v4u pq[2][2];
    {
      f32x16 pqq = logits(acc[0]);
#pragma unroll
      for (int hd = 0; hd < 2; ++hd) {
        float p[8];
#pragma unroll
        for (int j = 0; j < 8; ++j) p[j] = pqq[8 * hd + j];
        dual_softmax(p, cexp, pq[hd][0], pq[hd][1]);
      }
    }
    if (w == 0) {
      poll64(rsW, WS_FLAGS + xcd * FL_REP + F2 + hg * 128 + (lane & 15) * 8, true, tlo, thi);
      fin_glds(2); fin_glds(3);
    }
    BAR();
    STAMP(4);
    {
      const u32 un = w & 3, hf = w >> 2;
      v4u pr[8], pz[2];
#pragma unroll
      for (int i = 0; i < 8; ++i) { pr[i] = zero4; ld128_sc1(pr[i], lane16, rsW, WS_KVR + ((hg * 4 + un) * 16 + hf * 8 + i) * 1024); }
      pz[0] = zero4; pz[1] = zero4;
      const u32 zo = w >= 6 ? WS_KVZ + (hg * 2 + (w - 6)) * 2048 : OOB;
      ld128_sc1(pz[0], lane16, rsW, zo);
      ld128_sc1(pz[1], lane16, rsW, zo + 1024);
      fin_glds(4); fin_glds(5);
      asm volatile("s_waitcnt vmcnt(4)" : "+v"(pr[0]), "+v"(pr[1]), "+v"(pr[2]), "+v"(pr[3]), "+v"(pr[4]), "+v"(pr[5]), "+v"(pr[6]), "+v"(pr[7]), "+v"(pz[0]), "+v"(pz[1])::"memory");
      float sm[8];
      u32 klo = 0x00003f80u;
      asm("" : "+s"(klo));
#pragma unroll
      for (int j = 0; j < 8; ++j) sm[j] = 0.f;
#pragma unroll
      for (int i = 0; i < 8; ++i) {
        u32 d0 = pr[i].x, d1 = pr[i].y, d2 = pr[i].z, d3 = pr[i].w;
        sm[0] = dot2(d0, klo, sm[0]); sm[1] = dot2(d0, 0x3f800000u, sm[1]);
        sm[2] = dot2(d1, klo, sm[2]); sm[3] = dot2(d1, 0x3f800000u, sm[3]);
        sm[4] = dot2(d2, klo, sm[4]); sm[5] = dot2(d2, 0x3f800000u, sm[5]);
        sm[6] = dot2(d3, klo, sm[6]); sm[7] = dot2(d3, 0x3f800000u, sm[7]);
      }
      const u32 SUM = 98304;
      sts128f(SUM + (hf * 4 + un) * 2048 + lane16, f32x4{sm[0], sm[1], sm[2], sm[3]});
      sts128f(SUM + (hf * 4 + un) * 2048 + 1024 + lane16, f32x4{sm[4], sm[5], sm[6], sm[7]});
      if (w >= 6) {
        float z[8];
        u32 a[4] = {pz[0].x, pz[0].y, pz[0].z, pz[0].w}, c[4] = {pz[1].x, pz[1].y, pz[1].z, pz[1].w};
#pragma unroll
        for (int j = 0; j < 4; ++j) { z[2 * j] = blo(a[j]) + blo(c[j]); z[2 * j + 1] = bhi(a[j]) + bhi(c[j]); }
#pragma unroll
        for (int j = 0; j < 8; ++j) { z[j] += dpp<0x128>(z[j]); z[j] = x32sum(x16sum(z[j])); }
        if (lane < 8) sts128(SUM + 16384 + (w - 6) * 128 + lane16, v4u{pk(z[0], z[1]), pk(z[2], z[3]), pk(z[4], z[5]), pk(z[6], z[7])});
      }
      BAR();
      f32x16 o0, o1;
#pragma unroll
      for (int j = 0; j < 16; ++j) { o0[j] = 0.f; o1[j] = 0.f; }
#pragma unroll
      for (int s = 0; s < 2; ++s) {
        u32 a = SUM + (hp * 2 + s) * 2048 + lane16;
        f32x4 r0 = lds128f(a) + lds128f(a + 8192), r1 = lds128f(a + 1024) + lds128f(a + 8192 + 1024);
        v4u rf = {pk(r0[0], r0[1]), pk(r0[2], r0[3]), pk(r1[0], r1[1]), pk(r1[2], r1[3])};
        v4u zf = lds128(SUM + 16384 + hp * 128 + (((lo16 ? 2 : 0) + hh) * 2 + s) * 16);
        o0 = mfma(sel4(lo16, rf, zf), pq[0][s], o0);
        o1 = mfma(sel4(lo16, zf, rf), pq[1][s], o1);
      }
      const float i0 = __builtin_amdgcn_rcpf(o0[8] + 1e-6f), i1 = __builtin_amdgcn_rcpf(o1[0] + 1e-6f);
      const u32 oo = WS_O + ((rt * 4 + tg) * 32 + hg * 4 + hp * 2) * 1024 + lane16;
      st128_sc1(v4u{pk(o0[0] * i0, o0[1] * i0), pk(o0[2] * i0, o0[3] * i0), pk(o0[4] * i0, o0[5] * i0), pk(o0[6] * i0, o0[7] * i0)}, rsW, oo);
      st128_sc1(v4u{pk(o1[8] * i1, o1[9] * i1), pk(o1[10] * i1, o1[11] * i1), pk(o1[12] * i1, o1[13] * i1), pk(o1[14] * i1, o1[15] * i1)}, rsW, oo + 1024);
    }
    DRAIN_BAR();
    if (w == 0) publish(rsW, F3 + (rt * 8 + hg) * 8, tlo, thi, lane);
    STAMP(5);
  } else {
    v2u ep = {0u, 0u};
    ld64_sc1(ep, 0u, rsW, WS_EPOCH);
    const u32 bid = xcd * 16 + li;
    const u32 d64 = (xcd & 1) * 4 + (li & 3), ft = (xcd >> 1) * 4 + (li >> 2);
    const u32 g0 = (ft * 8) / 3, gown = (ft == 15 ? 43u : ((ft + 1) * 8) / 3) - g0;
    const u32 rot = (((li & 3) + (li >> 2)) * 8) & 31;
    const rsrc_t rs1 = mkrs(gw1, 2048 * 1376 * 2);
    v4u xr[2] = {{0u, 0u, 0u, 0u}, {0u, 0u, 0u, 0u}};
    ld128_pl(xr[0], lane16, rsX, (bid * 16 + 2 * w) * 1024);
    ld128_pl(xr[1], lane16, rsX, (bid * 16 + 2 * w + 1) * 1024);
    u32 wv[3];
#pragma unroll
    for (int i = 0; i < 3; ++i) { u32 o = i * 1024 + lane16, row = o / 192; wv[i] = row * 2752 + (o - row * 192); }
    auto b_stage = [&](u32 st, u32 slot) {
      u32 ks = kq * 32 + ((2 * st + dh + rot) & 31);
      u32 so = st < 16 ? ks * (16 * 2752) + g0 * 64 : OOB;
      u32 la = slot * 24576 + kq * 6144 + dh * 3072;
      glds(la, wv[0], rs1, so); glds(la + 1024, wv[1], rs1, so); glds(la + 2048, wv[2], rs1, so);
    };
    b_stage(0, 0);
    v2u bias[2][2];
#pragma unroll
    for (int i = 0; i < 2; ++i) {
      u32 fr = (i == 0) ? w : 8 + (w & 3), gs = fr >= 6 ? fr - 6 : fr;
      const u16* p = gb1 + g0 * 32 + gs * 16 + 4 * hh;
      bias[i][0] = *(const v2u*)p; bias[i][1] = *(const v2u*)(p + 8);
    }
    asm volatile("s_waitcnt vmcnt(7)" : "+v"(xr[0]), "+v"(xr[1]), "+v"(ep)::"memory");
    STAMP(1);
    b_stage(1, 1);
    tlo = rfl(ep.x) + 1u; thi = rfl(ep.y);
    const u32 tB = tlo * 0x9E3779B1u + thi;
    const u32 HIM = 147456;
    float mu[2], rs_[2];
#pragma unroll
    for (int r = 0; r < 2; ++r) {
      u32 d[4] = {xr[r].x, xr[r].y, xr[r].z, xr[r].w};
      float v[8];
#pragma unroll
      for (int j = 0; j < 4; ++j) { v[2 * j] = blo(d[j]); v[2 * j + 1] = bhi(d[j]); }
      float m = wsum(((v[0] + v[1]) + (v[2] + v[3])) + ((v[4] + v[5]) + (v[6] + v[7]))) * (1.f / 512.f);
      float ss = 0.f;
#pragma unroll
      for (int j = 0; j < 8; ++j) { v[j] -= m; ss = fmaf(v[j], v[j], ss); }
      float r1 = __builtin_amdgcn_rsqf(wsum(ss) * (1.f / 512.f) + 1e-5f);
      float s2 = 0.f, q2 = 0.f;
#pragma unroll
      for (int j = 0; j < 8; ++j) { v[j] *= r1; s2 += v[j]; q2 = fmaf(v[j], v[j], q2); }
      float m2 = wsum(s2) * (1.f / 512.f);
      float v2 = fmaxf(wsum(q2) * (1.f / 512.f) - m2 * m2, 0.f);
      float r2 = __builtin_amdgcn_rsqf(v2 + 1e-5f), nb = -m2 * r2;
      const u32 rr = 2 * w + r;
      sts128(HIM + rr * 1024 + ((lane ^ ((rr & 3) << 2)) << 4),
             v4u{pk(fmaf(v[0], r2, nb), fmaf(v[1], r2, nb)), pk(fmaf(v[2], r2, nb), fmaf(v[3], r2, nb)), pk(fmaf(v[4], r2, nb), fmaf(v[5], r2, nb)), pk(fmaf(v[6], r2, nb), fmaf(v[7], r2, nb))});
      mu[r] = m; rs_[r] = r1;
    }
    if (lane < 2) st128_sc1(v4u{__float_as_uint(lane ? mu[1] : mu[0]), tlo, __float_as_uint(lane ? rs_[1] : rs_[0]), tB}, rsW, WS_STATS + (bid * 16 + 2 * w + lane) * 16);
    BAR();
#pragma unroll
    for (int i = 0; i < 2; ++i) {
      const u32 d32 = 2 * w + i;
      const u32 a = HIM + (8 * hh + q4) * 1024 + (((d32 * 4 + 2 * blk + (p4 >> 1)) ^ (q4 << 2)) << 4) + (p4 & 1) * 8;
      st128_sc1(trfrag(a, a + 4096), rsW, WS_H + (d32 * 128 + bid) * 1024 + lane16);
    }
    DRAIN_BAR();
    if (w == 0) {
      publish(rsW, F1 + bid * 8, tlo, thi, lane);
      STAMP(2);
      poll128(rsW, WS_FLAGS + xcd * FL_REP + F1 + lane16, tlo, thi);
    }
    b_stage(2, 2); b_stage(3, 3); b_stage(4, 4);
    BAR();
    STAMP(3);
    const u32 hbase = WS_H + ((d64 * 2 + dh) * 128 + kq * 32) * 1024;
    v4u hf[8];
#pragma unroll
    for (int i = 0; i < 8; ++i) { hf[i] = v4u{0u, 0u, 0u, 0u}; ld128_sc1(hf[i], lane16, rsW, hbase + ((i + rot) & 31) * 1024); }
    f32x16 acc[3];
#pragma unroll
    for (int i = 0; i < 3; ++i)
#pragma unroll
      for (int j = 0; j < 16; ++j) acc[i][j] = 0.f;
    const u32 wa = kq * 6144 + (8 * hh + q4) * 192 + blk * 32 + p4 * 8;
    u32 slot = 0;
#pragma unroll 1
    for (u32 it0 = 0; it0 < 16; it0 += 4) {
#pragma unroll
      for (int u = 0; u < 4; ++u) {
        const u32 it = it0 + u;
        asm volatile("s_waitcnt vmcnt(6) lgkmcnt(0)\n\ts_barrier" : "+v"(hf[2 * u]), "+v"(hf[2 * u + 1])::"memory");
        b_stage(it + 5, slot == 0 ? 5u : slot - 1);
        const u32 sb = slot * 24576 + wa;
#pragma unroll
        for (int s2 = 0; s2 < 2; ++s2) {
#pragma unroll
          for (int g = 0; g < 3; ++g) acc[g] = mfma(trfrag(sb + s2 * 3072 + g * 64, sb + s2 * 3072 + 768 + g * 64), hf[2 * u + s2], acc[g]);
        }
#pragma unroll
        for (int s2 = 0; s2 < 2; ++s2) {
          u32 n = 2 * it + 8 + s2;
          ld128_sc1(hf[2 * u + s2], lane16, rsW, n < 32 ? hbase + ((n + rot) & 31) * 1024 : OOB);
        }
        slot = slot == 5 ? 0u : slot + 1;
      }
    }
    asm volatile("s_waitcnt vmcnt(0) lgkmcnt(0)\n\ts_barrier" : "+v"(hf[0]), "+v"(hf[1]), "+v"(hf[2]), "+v"(hf[3]), "+v"(hf[4]), "+v"(hf[5]), "+v"(hf[6]), "+v"(hf[7])::"memory");
    STAMP(4);
    fin_glds(0); fin_glds(1); fin_glds(2);
    const u32 RED = 49152;
#pragma unroll
    for (int g = 0; g < 3; ++g)
#pragma unroll
      for (int qq = 0; qq < 4; ++qq)
        sts128f(RED + (w * 12 + g * 4 + qq) * 1024 + lane16, f32x4{acc[g][4 * qq], acc[g][4 * qq + 1], acc[g][4 * qq + 2], acc[g][4 * qq + 3]});
    BAR();
#pragma unroll
    for (int i = 0; i < 2; ++i) {
      if (i == 0 || w < 4) {
        const u32 fr = (i == 0) ? w : 8 + w, fd = fr >= 6 ? 1u : 0u, gs = fr - 6 * fd;
        f32x4 s0 = {0.f, 0.f, 0.f, 0.f}, s1 = s0;
#pragma unroll
        for (int k = 0; k < 4; ++k) {
          u32 a = RED + ((k * 2 + fd) * 12 + gs * 2) * 1024 + lane16;
          s0 += lds128f(a); s1 += lds128f(a + 1024);
        }
        u32 bb[4] = {bias[i][0].x, bias[i][0].y, bias[i][1].x, bias[i][1].y};
        float v[8] = {s0[0] + blo(bb[0]), s0[1] + bhi(bb[0]), s0[2] + blo(bb[1]), s0[3] + bhi(bb[1]),
                      s1[0] + blo(bb[2]), s1[1] + bhi(bb[2]), s1[2] + blo(bb[3]), s1[3] + bhi(bb[3])};
#pragma unroll
        for (int j = 0; j < 8; ++j) {
          float t = v[j] * fmaf(v[j] * v[j], -2.f * 0.7978845608028654f * 0.044715f * LOG2E, -2.f * 0.7978845608028654f * LOG2E);
          v[j] = v[j] * __builtin_amdgcn_rcpf(1.f + __builtin_amdgcn_exp2f(t));
        }
        if ((gs >> 1) < gown)
          st128_sc1(v4u{pk(v[0], v[1]), pk(v[2], v[3]), pk(v[4], v[5]), pk(v[6], v[7])}, rsW, WS_U + ((d64 * 2 + fd) * 86 + g0 * 2 + gs) * 1024 + lane16);
      }
    }
    DRAIN_BAR();
    if (w == 0) publish(rsW, F4 + (d64 * 16 + ft) * 8, tlo, thi, lane);
    STAMP(5);
  }

  if (w == 0) {
    u32 off = WS_FLAGS + xcd * FL_REP + (lane < 16 ? F4 + dt * 128 + lane * 8 : F3 + (tt >> 1) * 64 + (lane & 7) * 8);
    poll64(rsW, off, true, tlo, thi);
    if (b == 0 && lane == 0) st64_sc1(v2u{tlo, thi}, rsW, WS_EPOCH);
  }
#pragma unroll 1
  for (u32 s = fin_done; s < 8; ++s) fin_glds(s);
  const u32 etok = tid >> 3, ec = tid & 7;
  v4u xres = {0u, 0u, 0u, 0u};
  ld128_pl(xres, ((tt * 64 + etok) * 512 + dt * 64 + ec * 8) * 2, rsX, 0u);
  const float b2v = __uint_as_float((u32)gb2[tt * 64 + etok] << 16);
  BAR();
  STAMP(6);
  const u32 abase = isWO ? WS_O + (tt * 2 + dh) * 32 * 1024 : WS_U + ((dt * 2 + dh) * 86 + fbeg) * 1024;
  v4u af[8];
#pragma unroll
  for (int i = 0; i < 8; ++i) {
    u32 n = (i + frot) & 31;
    af[i] = v4u{0u, 0u, 0u, 0u};
    ld128_sc1(af[i], lane16, rsW, n < fcnt ? abase + n * 1024 : OOB);
  }
  f32x16 acc[2];
#pragma unroll
  for (int i = 0; i < 2; ++i)
#pragma unroll
    for (int j = 0; j < 16; ++j) acc[i][j] = 0.f;
  const u32 fa = kq * 4096 + (4 * hh + q4) * 128 + ((q4 >> 1) << 6) + blk * 32 + p4 * 8;
  u32 slot = 0;
#pragma unroll 1
  for (u32 it0 = 0; it0 < 16; it0 += 4) {
#pragma unroll
    for (int u = 0; u < 4; ++u) {
      const u32 it = it0 + u;
      asm volatile("s_waitcnt vmcnt(6) lgkmcnt(0)\n\ts_barrier" : "+v"(af[2 * u]), "+v"(af[2 * u + 1])::"memory");
      fin_glds(it + 8);
      const u32 s0 = slot * 16384 + fa, s1 = s0 ^ 64;
      v4u wf[2][2];
#pragma unroll
      for (int s2 = 0; s2 < 2; ++s2) {
        wf[s2][0] = trfrag(s0 + s2 * 2048, s0 + s2 * 2048 + 1024);
        wf[s2][1] = trfrag(s1 + s2 * 2048, s1 + s2 * 2048 + 1024);
      }
      if (isWO) {
#pragma unroll
        for (int s2 = 0; s2 < 2; ++s2) { acc[0] = mfma(wf[s2][0], af[2 * u + s2], acc[0]); acc[1] = mfma(wf[s2][1], af[2 * u + s2], acc[1]); }
      } else {
#pragma unroll
        for (int s2 = 0; s2 < 2; ++s2) { acc[0] = mfma(af[2 * u + s2], wf[s2][0], acc[0]); acc[1] = mfma(af[2 * u + s2], wf[s2][1], acc[1]); }
      }
#pragma unroll
      for (int s2 = 0; s2 < 2; ++s2) {
        u32 m = 2 * it + 8 + s2, n = (m + frot) & 31;
        ld128_sc1(af[2 * u + s2], lane16, rsW, (m < 32 && n < fcnt) ? abase + n * 1024 : OOB);
      }
      slot = slot == 8 ? 0u : slot + 1;
    }
  }
  asm volatile("s_waitcnt vmcnt(0) lgkmcnt(0)\n\ts_barrier" : "+v"(af[0]), "+v"(af[1]), "+v"(af[2]), "+v"(af[3]), "+v"(af[4]), "+v"(af[5]), "+v"(af[6]), "+v"(af[7]), "+v"(xres)::"memory");
  STAMP(7);
  {
    const u32 pb = kq * 16384 + (isWO ? (dh * 32 + l31) * 256 : l31 * 256 + dh * 128);
    const u32 pi = isWO ? 128u : 8192u;
#pragma unroll
    for (int i = 0; i < 2; ++i)
#pragma unroll
      for (int qq = 0; qq < 4; ++qq)
        sts128f(pb + i * pi + (((2 * qq + hh) ^ (l31 & 7)) << 4), f32x4{acc[i][4 * qq], acc[i][4 * qq + 1], acc[i][4 * qq + 2], acc[i][4 * qq + 3]});
  }
  BAR();
  {
    const u32 a0 = etok * 256 + (((2 * ec) ^ (etok & 7)) << 4), a1 = a0 ^ 16;
    f32x4 y0 = lds128f(a0) + lds128f(a0 + 16384) + lds128f(a0 + 32768);
    f32x4 y1 = lds128f(a1) + lds128f(a1 + 16384) + lds128f(a1 + 32768);
    f32x4 s0 = lds128f(a0 + 49152), s1 = lds128f(a1 + 49152);
    u32 xd[4] = {xres.x, xres.y, xres.z, xres.w};
    float o[8];
#pragma unroll
    for (int j = 0; j < 8; ++j) {
      float y = (j < 4 ? y0[j & 3] : y1[j & 3]) + b2v, s = j < 4 ? s0[j & 3] : s1[j & 3];
      float g = y * __builtin_amdgcn_rcpf(1.f + __builtin_amdgcn_exp2f(-LOG2E * y));
      o[j] = fmaf(s, g, (j & 1) ? bhi(xd[j >> 1]) : blo(xd[j >> 1]));
    }
    *(v4u*)(gout + (tt * 64 + etok) * 512 + dt * 64 + ec * 8) = v4u{pk(o[0], o[1]), pk(o[2], o[3]), pk(o[4], o[5]), pk(o[6], o[7])};
  }
}

extern "C" void kernel_launch(void* const* d_in, const int* in_sizes, int n_in, void* d_out, int out_size, void* d_ws, size_t ws_size,
                              hipStream_t stream) {
  (void)in_sizes; (void)n_in; (void)out_size; (void)ws_size;
  (void)hipFuncSetAttribute((const void*)fused_kernel, hipFuncAttributeMaxDynamicSharedMemorySize, LDS_BYTES);
  fused_kernel<<<256, 512, LDS_BYTES, stream>>>((const u16*)d_in[0], (const u16*)d_in[1], (const u16*)d_in[2], (const u16*)d_in[3],
                                                (const u16*)d_in[5], (const u16*)d_in[6], (const u16*)d_in[7], (const u16*)d_in[8],
                                                (u16*)d_out, (char*)d_ws);
}
```

```cpp
#include <hip/hip_runtime.h>
#include <stdint.h>

typedef unsigned u32;
typedef unsigned short u16;
typedef unsigned long long u64;
typedef u32 v4u __attribute__((ext_vector_type(4)));
typedef u32 v2u __attribute__((ext_vector_type(2)));
typedef float f32x16 __attribute__((ext_vector_type(16)));
typedef float f32x4 __attribute__((ext_vector_type(4)));
typedef float f32x2 __attribute__((ext_vector_type(2)));
typedef short s16x4 __attribute__((ext_vector_type(4)));
typedef __bf16 bf16x8 __attribute__((ext_vector_type(8)));
typedef __bf16 bf16x2 __attribute__((ext_vector_type(2)));
#define LAS __attribute__((address_space(3)))
#define DI __device__ __forceinline__

#define HOLD 1
#define NO_TOUCH_X
#define STAMPS 0

constexpr u32 WS_EPOCH = 0, WS_FLAGS = 4096, FL_REP = 4096;
constexpr u32 F1 = 0, F2 = 1024, F3 = 2048, F4 = 3072;
constexpr u32 WS_STATS = 65536;
constexpr u32 WS_KVZ = 131072;
constexpr u32 WS_KVR = 262144;
constexpr u32 WS_H = 1u << 20;
constexpr u32 WS_O = 3u << 20;
constexpr u32 WS_U = 5u << 20;
constexpr u32 WS_BYTES = 7u << 20;
constexpr u32 WS_STAMP = 8u << 20;
constexpr u32 LDS_BYTES = 163840;
constexpr u32 CODE_BYTES = 0;
constexpr u32 TOUCH_W = 3;
constexpr u32 SPIN_CAP = 1u << 15;
constexpr float LOG2E = 1.4426950408889634f;

DI u32 rfl(u32 v) { return (u32)__builtin_amdgcn_readfirstlane((int)v); }
DI v4u mkrs(const void* p, u32 bytes) { u64 a = (u64)p; return v4u{(u32)a, (u32)(a >> 32) & 0xffffu, bytes, 0x00020000u}; }
DI __amdgpu_buffer_rsrc_t mkrsb(const void* p, u32 bytes) { return __builtin_amdgcn_make_buffer_rsrc((void*)p, (short)0, (int)bytes, 0x00020000); }
DI void gl4(u32 m, u32 v0, u32 v1, u32 v2, u32 v3, v4u rs, u32 sa, u32 sb) {
  asm volatile("s_mov_b32 m0, %0\n\ts_nop 0\n\t"
               "buffer_load_dwordx4 %1, %5, %6 offen lds\n\tbuffer_load_dwordx4 %2, %5, %6 offen offset:1024 lds\n\t"
               "buffer_load_dwordx4 %3, %5, %7 offen offset:2048 lds\n\tbuffer_load_dwordx4 %4, %5, %7 offen offset:3072 lds"
               ::"s"(m), "v"(v0), "v"(v1), "v"(v2), "v"(v3), "s"(rs), "s"(sa), "s"(sb) : "memory");
}
DI void gl3(u32 m, u32 v0, u32 v1, u32 v2, v4u rs, u32 sa) {
  asm volatile("s_mov_b32 m0, %0\n\ts_nop 0\n\t"
               "buffer_load_dwordx4 %1, %4, %5 offen lds\n\tbuffer_load_dwordx4 %2, %4, %5 offen offset:1024 lds\n\t"
               "buffer_load_dwordx4 %3, %4, %5 offen offset:2048 lds"
               ::"s"(m), "v"(v0), "v"(v1), "v"(v2), "s"(rs), "s"(sa) : "memory");
}
DI void ld128_sc1(v4u& d, u32 voff, v4u rs, u32 soff) {
  asm volatile("buffer_load_dwordx4 %0, %1, %2, %3 offen sc1" : "+v"(d) : "v"(voff), "s"(rs), "s"(soff) : "memory");
}
DI void ld128_sc1_new(v4u& d, u32 voff, v4u rs, u32 soff) {
  asm volatile("buffer_load_dwordx4 %0, %1, %2, %3 offen sc1" : "=v"(d) : "v"(voff), "s"(rs), "s"(soff) : "memory");
}
DI void ld128_new(v4u& d, u32 voff, v4u rs, u32 soff) {
  asm volatile("buffer_load_dwordx4 %0, %1, %2, %3 offen" : "=v"(d) : "v"(voff), "s"(rs), "s"(soff) : "memory");
}
DI void ld64_sc1_new(v2u& d, u32 voff, v4u rs, u32 soff) {
  asm volatile("buffer_load_dwordx2 %0, %1, %2, %3 offen sc1" : "=v"(d) : "v"(voff), "s"(rs), "s"(soff) : "memory");
}
DI void st128_sc1(v4u v, __amdgpu_buffer_rsrc_t rs, u32 off) { __builtin_amdgcn_raw_buffer_store_b128(v, rs, (int)off, 0, 16); }
DI void st64_sc1(v2u v, __amdgpu_buffer_rsrc_t rs, u32 off) { __builtin_amdgcn_raw_buffer_store_b64(v, rs, (int)off, 0, 16); }
#define BAR() asm volatile("s_waitcnt lgkmcnt(0)\n\ts_barrier" ::: "memory")
#define DRAIN_BAR() asm volatile("s_waitcnt vmcnt(0) lgkmcnt(0)\n\ts_barrier" ::: "memory")
#define VBAR(n) asm volatile("s_waitcnt vmcnt(" #n ")\n\ts_barrier" ::: "memory")
#define LBAR() asm volatile("s_barrier" ::: "memory")
#define SCHED() __builtin_amdgcn_sched_barrier(0)

DI v2u trrd(u32 a) { return __builtin_bit_cast(v2u, __builtin_amdgcn_ds_read_tr16_b64_v4i16((LAS s16x4*)(size_t)a)); }
DI v4u trfrag(u32 a0, u32 a1) { v2u x = trrd(a0), y = trrd(a1); return v4u{x.x, x.y, y.x, y.y}; }
DI v4u lds128(u32 a) { return *(LAS v4u*)(size_t)a; }
DI v2u lds64(u32 a) { return *(LAS v2u*)(size_t)a; }
DI f32x4 lds128f(u32 a) { return *(LAS f32x4*)(size_t)a; }
DI void sts128(u32 a, v4u v) { *(LAS v4u*)(size_t)a = v; }
DI void sts128f(u32 a, f32x4 v) { *(LAS f32x4*)(size_t)a = v; }
DI void sts64(u32 a, v2u v) { *(LAS v2u*)(size_t)a = v; }
DI void sts32f(u32 a, float v) { *(LAS float*)(size_t)a = v; }
DI f32x16 mfma(v4u a, v4u b, f32x16 c) {
  return __builtin_amdgcn_mfma_f32_32x32x16_bf16(__builtin_bit_cast(bf16x8, a), __builtin_bit_cast(bf16x8, b), c, 0, 0, 0);
}
DI f32x4 mfma16(v4u a, v4u b, f32x4 c) {
  return __builtin_amdgcn_mfma_f32_16x16x32_bf16(__builtin_bit_cast(bf16x8, a), __builtin_bit_cast(bf16x8, b), c, 0, 0, 0);
}
DI u32 pk2(f32x2 v) { return __builtin_bit_cast(u32, __builtin_convertvector(v, bf16x2)); }
DI u32 pk(float a, float b) { return pk2(f32x2{a, b}); }
DI float blo(u32 v) { return __uint_as_float(v << 16); }
DI float bhi(u32 v) { return __uint_as_float(v & 0xffff0000u); }
DI f32x2 unp(u32 v) { return f32x2{blo(v), bhi(v)}; }
DI f32x2 fma2(f32x2 a, f32x2 b, f32x2 c) { return __builtin_elementwise_fma(a, b, c); }
DI f32x2 spl(float a) { return f32x2{a, a}; }
DI float dot2(u32 a, u32 b, float c) { return __builtin_amdgcn_fdot2_f32_bf16(__builtin_bit_cast(bf16x2, a), __builtin_bit_cast(bf16x2, b), c, false); }
template <int C> DI float dpp(float v) { return __int_as_float(__builtin_amdgcn_mov_dpp(__float_as_int(v), C, 0xF, 0xF, true)); }
DI float x16sum(float v) { u32 x = __float_as_uint(v); auto r = __builtin_amdgcn_permlane16_swap(x, x, false, false); return __uint_as_float(r[0]) + __uint_as_float(r[1]); }
DI float x32sum(float v) { u32 x = __float_as_uint(v); auto r = __builtin_amdgcn_permlane32_swap(x, x, false, false); return __uint_as_float(r[0]) + __uint_as_float(r[1]); }
DI float x32max(float v) { u32 x = __float_as_uint(v); auto r = __builtin_amdgcn_permlane32_swap(x, x, false, false); return fmaxf(__uint_as_float(r[0]), __uint_as_float(r[1])); }
DI float x32min(float v) { u32 x = __float_as_uint(v); auto r = __builtin_amdgcn_permlane32_swap(x, x, false, false); return fminf(__uint_as_float(r[0]), __uint_as_float(r[1])); }
DI float hsum32(float v) {
  v += dpp<0xB1>(v); v += dpp<0x4E>(v); v += dpp<0x141>(v); v += dpp<0x140>(v);
  return x16sum(v);
}
DI f32x16 zero16() { f32x16 z;
#pragma unroll
  for (int j = 0; j < 16; ++j) z[j] = 0.f;
  return z; }

DI void publish(__amdgpu_buffer_rsrc_t rsW, u32 off, u32 tlo, u32 thi, u32 lane) {
  if (lane < 8) st64_sc1(v2u{tlo, thi}, rsW, WS_FLAGS + lane * FL_REP + off);
}
DI void poll64(v4u rsW, u32 off, u32 tlo, u32 thi) {
#pragma unroll 1
  for (u32 spin = 0; spin < SPIN_CAP; ++spin) {
    v2u t;
    asm volatile("buffer_load_dwordx2 %0, %1, %2, 0 offen sc1\n\ts_waitcnt vmcnt(0)" : "=&v"(t) : "v"(off), "s"(rsW) : "memory");
    if (__all(t.x == tlo && t.y == thi)) break;
  }
}
DI void poll128(v4u rsW, u32 off, u32 tlo, u32 thi) {
#pragma unroll 1
  for (u32 spin = 0; spin < SPIN_CAP; ++spin) {
    v4u t;
    asm volatile("buffer_load_dwordx4 %0, %1, %2, 0 offen sc1\n\ts_waitcnt vmcnt(0)" : "=&v"(t) : "v"(off), "s"(rsW) : "memory");
    if (__all(t.x == tlo && t.y == thi && t.z == tlo && t.w == thi)) break;
  }
}

DI void dual_softmax(const f32x16& z, int o, float c, v4u& fp, v4u& fn) {
  f32x2 p[4] = {{z[o], z[o + 1]}, {z[o + 2], z[o + 3]}, {z[o + 4], z[o + 5]}, {z[o + 6], z[o + 7]}};
  float mx = fmaxf(fmaxf(fmaxf(p[0].x, p[0].y), p[1].x), fmaxf(fmaxf(p[1].y, p[2].x), p[2].y));
  mx = fmaxf(fmaxf(mx, p[3].x), p[3].y);
  float mn = fminf(fminf(fminf(p[0].x, p[0].y), p[1].x), fminf(fminf(p[1].y, p[2].x), p[2].y));
  mn = fminf(fminf(mn, p[3].x), p[3].y);
  mx = x32max(mx); mn = x32min(mn);
  const f32x2 a = spl(-mx * c), b = spl(mn * c), c2 = spl(c), n2 = spl(-c);
  f32x2 e[4], g[4];
#pragma unroll
  for (int i = 0; i < 4; ++i) {
    f32x2 t = fma2(p[i], c2, a), u = fma2(p[i], n2, b);
    e[i] = f32x2{__builtin_amdgcn_exp2f(t.x), __builtin_amdgcn_exp2f(t.y)};
    g[i] = f32x2{__builtin_amdgcn_exp2f(u.x), __builtin_amdgcn_exp2f(u.y)};
  }
  f32x2 se = (e[0] + e[1]) + (e[2] + e[3]), sg = (g[0] + g[1]) + (g[2] + g[3]);
  const f32x2 ie = spl(__builtin_amdgcn_rcpf(x32sum(se.x + se.y))), ig = spl(__builtin_amdgcn_rcpf(x32sum(sg.x + sg.y)));
  fp = v4u{pk2(e[0] * ie), pk2(e[1] * ie), pk2(e[2] * ie), pk2(e[3] * ie)};
  fn = v4u{pk2(g[0] * ig), pk2(g[1] * ig), pk2(g[2] * ig), pk2(g[3] * ig)};
}

#define STAMP(i) do {} while (0)

#define TS(base, n) do {} while (0)
#define TSDUMP(base, sec) do {} while (0)
#define VBART(n, base, k) VBAR(n)
__global__ __launch_bounds__(768, 3) void fused_kernel(const u16* __restrict__ gx, const u16* __restrict__ gwqkv, const u16* __restrict__ gwphi,
                                                         const u16* __restrict__ gwo, const u16* __restrict__ gw1, const u16* __restrict__ gb1,
                                                         const u16* __restrict__ gw2, const u16* __restrict__ gb2, u16* __restrict__ gout, char* ws) {
  u64 pc0;
  asm volatile("s_getpc_b64 %0" : "=s"(pc0));
  STAMP(0);
  const u32 tid = threadIdx.x, lane = tid & 63;
  const u32 w = rfl(tid >> 6);
  const u32 b = blockIdx.x, xcd = b & 7, idx = b >> 3, li = idx >> 1;
  const bool roleA = (idx & 1) == 0;
  const u32 lane16 = lane * 16;
  const u32 tt = xcd * 4 + (idx >> 3), dt = idx & 7;
  const u32 frot = (((idx >> 3) + (dt >> 1)) * 8) & 31;
  const u32 rt = (xcd >> 1) * 4 + (li >> 2), hg = (xcd & 1) * 4 + (li & 3);
  const u32 bid = xcd * 16 + li;
  const u32 d64 = (xcd & 1) * 4 + (li & 3), ft = (xcd >> 1) * 4 + (li >> 2);
  const u32 g0 = (ft * 8) / 3;
  const u32 brot = (((li & 3) + (li >> 2)) * 8) & 31;

  if (w >= 8) {
    const u32 lw = w - 8;
    const v4u rsC = mkrs((const void*)(pc0 & ~(u64)255), CODE_BYTES);
    const u32 cvo = (li * 16 + lw * 4 + (lane & 3)) * 128 + (lane >> 2) * (64 * 1024);
    auto touch = [&](u32 la) {
      asm volatile("s_mov_b32 m0, %0\n\ts_nop 0\n\tbuffer_load_dword %1, %2, 0 offen lds" ::"s"(la), "v"(cvo), "s"(rsC) : "memory");
    };
    touch(roleA ? 150528u : 5 * 24576 + lw * 6144);
    const bool wo = lw == 3;
    const u32 fpitch = wo ? 1024u : 4096u, fsh = wo ? 14u : 16u;
    const u32 fcol = wo ? dt * 128 : tt * 128, fcnt = wo ? 32u : (lw == 2 ? 28u : 29u);
    const char* fbase = wo ? (const char*)gwo : (const char*)gw2 + (size_t)(lw * 29) * 65536;
    const v4u rsF = mkrs(fbase + fcol - 4096, (fcnt << fsh) - fcol + 4096);
    const u32 fv0 = (lane >> 3) * fpitch + (((lane & 7) ^ (((lane >> 4) & 1) << 2)) << 4) + 4096;
    const u32 fv1 = fv0 + 8 * fpitch - 1024, fv2 = fv0 - 2048, fv3 = fv1 - 2048;
    auto fin = [&](u32 st, u32 slot) {
      u32 i = (2 * st + frot) & 31;
      gl4(slot * 16384 + lw * 4096, fv0, fv1, fv2, fv3, rsF, i << fsh, (i + 1) << fsh);
    };
    if (roleA) {
      const v4u rsX = mkrs((const char*)gx - 4096, 2048 * 1024 + 4096), rsQ = mkrs((const char*)gwqkv - 4096, 512 * 3072 + 4096);
      const u32 rot = (2 * ((li >> 2) + (li & 3))) & 7;
      u32 xv[4], wv[6];
#pragma unroll
      for (int i = 0; i < 4; ++i) {
        u32 row = 32 * lw + 8 * i + (lane >> 3);
        xv[i] = (rt * 128 + row) * 1024 + (((lane & 7) ^ ((row >> 1) & 7)) << 4) + 4096 - i * 1024;
      }
#pragma unroll
      for (int i = 0; i < 3; ++i) {
        u32 o = i * 1024 + lane16, row = o / 384, c = (o - row * 384) >> 4;
        wv[i] = (16 * lw + row) * 3072 + (c >> 3) * 1024 + hg * 128 + (((c & 7) ^ (((row >> 1) & 1) << 2)) << 4) + 4096 - i * 1024;
        wv[3 + i] = wv[i] + 8 * 3072;
      }
      auto stage = [&](u32 st, u32 slot) {
        u32 k = (st + rot) & 7, la = slot * 40960;
        gl4(la + lw * 4096, xv[0], xv[1], xv[2], xv[3], rsX, k * 128, k * 128);
        gl3(la + 16384 + lw * 6144, wv[0], wv[1], wv[2], rsQ, k * (64 * 3072));
        gl3(la + 16384 + lw * 6144 + 3072, wv[3], wv[4], wv[5], rsQ, k * (64 * 3072));
      };
      stage(0, 0); stage(1, 1); stage(2, 2);
      VBAR(20);
      u32 slot = 0;
#pragma unroll 1
      for (u32 j = 0; j < 5; ++j) { VBART(10, 131072, j); stage(j + 3, slot); TS(131072, 3 * j + 2); slot = slot == 2 ? 0u : slot + 1; }
      touch(150528u);
      VBART(11, 131072, 5); VBART(1, 131072, 6); LBAR(); TS(131072, 22); TSDUMP(131072, 0);
      fin(0, 0); fin(1, 1);
      LBAR(); LBAR(); LBAR();
      touch(150528u);
      fin(2, 2); fin(3, 3);
      LBAR(); LBAR();
      fin(4, 4); fin(5, 5); fin(6, 6); fin(7, 7); fin(8, 8);
      LBAR();
    } else {
      const v4u rs1 = mkrs((const char*)gw1 + (size_t)(lw * 512) * 2752 + g0 * 64 - 4096, 512 * 2752 - g0 * 64 + 4096);
      u32 wv[3];
#pragma unroll
      for (int i = 0; i < 3; ++i) { u32 o = i * 1024 + lane16, row = o / 192; wv[i] = row * 2752 + (o - row * 192) + 4096 - i * 1024; }
      auto stage = [&](u32 st, u32 slot) {
        u32 so = ((2 * st + brot) & 31) * (16 * 2752), la = slot * 24576 + lw * 6144;
        gl3(la, wv[0], wv[1], wv[2], rs1, so);
        gl3(la + 3072, wv[0], wv[1], wv[2], rs1, so + 16 * 2752);
      };
      LBAR();
      LBAR();
      stage(0, 0); stage(1, 1); stage(2, 2); stage(3, 3); stage(4, 4); stage(5, 5);
      VBAR(30);
      u32 slot = 0;
#pragma unroll 1
      for (u32 j = 0; j < 10; ++j) { VBART(24, 155648, j); stage(j + 6, slot); TS(155648, 3 * j + 2); slot = slot == 5 ? 0u : slot + 1; }
      touch(150528u);
      VBART(25, 155648, 10); VBART(19, 155648, 11); VBART(13, 155648, 12); VBART(7, 155648, 13);
      fin(0, 0); fin(1, 1); fin(2, 2);
      VBART(13, 155648, 14); LBAR(); TS(155648, 46); TSDUMP(155648, 0);
      LBAR();
      {
        const __amdgpu_buffer_rsrc_t rsWb = mkrsb(ws, WS_BYTES);
        const u32 gown = (ft == 15 ? 43u : ((ft + 1) * 8) / 3) - g0;
        const u32 gs = 2 + lw, hh = lane >> 5;
        const u16* p = gb1 + g0 * 32 + gs * 16 + 4 * hh;
        const v2u b0 = *(const v2u*)p, b1 = *(const v2u*)(p + 8);
        f32x4 s0 = {0.f, 0.f, 0.f, 0.f}, s1 = s0;
#pragma unroll
        for (int k = 0; k < 4; ++k) {
          u32 a = 49152 + ((k * 2 + 1) * 12 + gs * 2) * 1024 + lane16;
          s0 += lds128f(a); s1 += lds128f(a + 1024);
        }
        f32x2 v[4] = {f32x2{s0[0], s0[1]} + unp(b0.x), f32x2{s0[2], s0[3]} + unp(b0.y), f32x2{s1[0], s1[1]} + unp(b1.x), f32x2{s1[2], s1[3]} + unp(b1.y)};
        v4u o;
#pragma unroll
        for (int j = 0; j < 4; ++j) {
          f32x2 t = v[j] * fma2(v[j] * v[j], spl(-2.f * 0.7978845608028654f * 0.044715f * LOG2E), spl(-2.f * 0.7978845608028654f * LOG2E));
          f32x2 d = f32x2{__builtin_amdgcn_exp2f(t.x), __builtin_amdgcn_exp2f(t.y)} + spl(1.f);
          o[j] = pk2(v[j] * f32x2{__builtin_amdgcn_rcpf(d.x), __builtin_amdgcn_rcpf(d.y)});
        }
        if ((gs >> 1) < gown) st128_sc1(o, rsWb, WS_U + ((d64 * 2 + 1) * 86 + g0 * 2 + gs) * 1024 + lane16);
      }
      DRAIN_BAR();
      fin(3, 3); fin(4, 4); fin(5, 5); fin(6, 6); fin(7, 7); fin(8, 8);
    }
    VBAR(32);
#pragma unroll 1
    for (u32 j = 0; j < 7; ++j) { VBART(28, 155648, j); fin(j + 9, j); TS(155648, 3 * j + 2); }
    touch(150528u);
    VBART(29, 155648, 7); VBART(25, 155648, 8); VBART(21, 155648, 9); VBART(17, 155648, 10); VBART(13, 155648, 11); VBART(9, 155648, 12); VBART(5, 155648, 13); VBART(1, 155648, 14);
    LBAR(); TS(155648, 46); TSDUMP(155648, 1); LBAR();
    return;
  }

  const u32 l31 = lane & 31, hh = lane >> 5, blk = (lane >> 4) & 1, q4 = (lane & 15) >> 2, p4 = lane & 3;
  const v4u rsW = mkrs(ws, WS_BYTES);
  const __amdgpu_buffer_rsrc_t rsWb = mkrsb(ws, WS_BYTES);
  const v4u rsX = mkrs(gx, 2048 * 1024);
  const u32 dh = w & 1, kq = w >> 1;
  u32 tlo, thi;

  if (roleA) {
    v2u ep;
    ld64_sc1_new(ep, 0u, rsW, WS_EPOCH);
    const u32 tg = w & 3, hp = w >> 2;
    v4u wph;
    {
      const u16* p = gwphi + (hg * 4 + hp * 2 + (l31 >> 4)) * 256 + (4 * hh) * 16 + (l31 & 15);
      wph.x = p[0] | ((u32)p[16] << 16); wph.y = p[32] | ((u32)p[48] << 16);
      wph.z = p[128] | ((u32)p[144] << 16); wph.w = p[160] | ((u32)p[176] << 16);
    }
    const u32 xa0 = (32 * tg + l31) * 128 + ((hh ^ ((l31 >> 1) & 7)) << 4);
    const u32 wa = 16384 + (8 * hh + q4) * 384 + ((hp ^ (q4 >> 1)) << 6) + blk * 32 + p4 * 8;
    f32x16 acc[3] = {zero16(), zero16(), zero16()};
    float cs[3] = {0.f, 0.f, 0.f};
    v4u stg = {0u, 0u, 0u, 0u};
    const u32 stoff = WS_STATS + (rt * 128 + 32 * tg + l31) * 16;
    v4u xf[2], wf[2][3];
    auto rd = [&](int buf, u32 sb, int ks) {
      xf[buf] = lds128(sb + (xa0 ^ (ks << 5)));
#pragma unroll
      for (int s = 0; s < 3; ++s) wf[buf][s] = trfrag(sb + wa + ks * 6144 + s * 128, sb + wa + ks * 6144 + 1536 + s * 128);
    };
    BAR();
    STAMP(1);
    rd(0, 0, 0);
    u32 sb = 0;
#pragma unroll 1
    for (u32 st = 0; st < 8; ++st) {
#pragma unroll
      for (int ks = 0; ks < 4; ++ks) {
        const int c = ks & 1;
        if (ks == 3) { BAR(); sb = sb == 81920 ? 0u : sb + 40960; rd(c ^ 1, sb, 0); }
        else rd(c ^ 1, sb, ks + 1);
        SCHED();
#pragma unroll
        for (int s = 0; s < 3; ++s) acc[s] = mfma(wf[c][s], xf[c], acc[s]);
        if (tg == (u32)ks) {
#pragma unroll
          for (int s = 0; s < 3; ++s) {
            u32 a0 = wf[c][s].x, a1 = wf[c][s].y, a2 = wf[c][s].z, a3 = wf[c][s].w;
            cs[s] = dot2(a0, 0x3f803f80u, cs[s]); cs[s] = dot2(a1, 0x3f803f80u, cs[s]);
            cs[s] = dot2(a2, 0x3f803f80u, cs[s]); cs[s] = dot2(a3, 0x3f803f80u, cs[s]);
          }
        }
        if (ks == 0 && st == 7) ld128_sc1(stg, stoff, rsW, 0u);
        SCHED();
      }
    }
    STAMP(2);
    const u32 CSP = 147456;
#pragma unroll
    for (int s = 0; s < 3; ++s) sts32f(CSP + (hp * 3 + s) * 512 + l31 * 16 + tg * 4, x32sum(cs[s]));
    asm volatile("s_waitcnt vmcnt(0) lgkmcnt(0)\n\ts_barrier" : "+v"(stg), "+v"(ep)::"memory");
    tlo = rfl(ep.x) + 1u; thi = rfl(ep.y);
    const u32 tB = tlo * 0x9E3779B1u + thi;
    v4u ca[3];
#pragma unroll
    for (int s = 0; s < 3; ++s) {
      f32x4 c4 = lds128f(CSP + (hp * 3 + s) * 512 + l31 * 16);
      float c = (c4[0] + c4[1]) + (c4[2] + c4[3]);
      u32 hi = pk(c, c);
      ca[s] = v4u{pk(c, c - blo(hi)), hi, 0u, 0u};
    }
#pragma unroll 1
    for (u32 spin = 0; spin < SPIN_CAP; ++spin) {
      if (__all(stg.y == tlo && stg.w == tB)) break;
      asm volatile("buffer_load_dwordx4 %0, %1, %2, 0 offen sc1\n\ts_waitcnt vmcnt(0)" : "+v"(stg) : "v"(stoff), "s"(rsW) : "memory");
    }
    const float rstd = __uint_as_float(stg.z), cexp = rstd * LOG2E;
    {
      const float nm = hh ? 0.f : -__uint_as_float(stg.x);
      const u32 mh = pk(nm, nm);
      const v4u mb = {mh, pk(nm - blo(mh), 0.f), 0u, 0u};
      acc[1] = mfma(ca[1], mb, acc[1]); acc[2] = mfma(ca[2], mb, acc[2]); acc[0] = mfma(ca[0], mb, acc[0]);
    }
    const bool lo16 = l31 < 16;
    const v4u wph0 = {lo16 ? wph.x : 0u, lo16 ? wph.y : 0u, lo16 ? wph.z : 0u, lo16 ? wph.w : 0u};
    const v4u wph1 = {wph.x ^ wph0.x, wph.y ^ wph0.y, wph.z ^ wph0.z, wph.w ^ wph0.w};
    auto logits = [&](const f32x16& a) {
      v4u f0 = {pk(a[0], a[1]), pk(a[2], a[3]), pk(a[4], a[5]), pk(a[6], a[7])};
      v4u f1 = {pk(a[8], a[9]), pk(a[10], a[11]), pk(a[12], a[13]), pk(a[14], a[15])};
      return mfma(wph1, f1, mfma(wph0, f0, zero16()));
    };
    const u32 IMG = 98304;
    {
      f32x16 pkk = logits(acc[1]);
      const u32 sw = ((l31 >> 1) & 1) | ((((l31 >> 2) ^ (l31 >> 3)) & 1) << 1);
      const u32 wa2 = IMG + w * 6144 + l31 * 64 + hh * 8 + (sw << 4);
      const f32x2 r2 = spl(rstd);
#pragma unroll
      for (int qq = 0; qq < 4; ++qq)
        sts64((wa2 ^ (qq << 4)) + 4096, v2u{pk2(f32x2{acc[2][4 * qq], acc[2][4 * qq + 1]} * r2), pk2(f32x2{acc[2][4 * qq + 2], acc[2][4 * qq + 3]} * r2)});
#pragma unroll
      for (int hd = 0; hd < 2; ++hd) {
        v4u fp, fn;
        dual_softmax(pkk, 8 * hd, cexp, fp, fn);
        sts64((wa2 ^ 0) + hd * 2048, v2u{fp.x, fp.y});
        sts64((wa2 ^ 16) + hd * 2048, v2u{fp.z, fp.w});
        sts64((wa2 ^ 32) + hd * 2048, v2u{fn.x, fn.y});
        sts64((wa2 ^ 48) + hd * 2048, v2u{fn.z, fn.w});
      }
    }
    STAMP(9);
    BAR();
    const v4u ones = {0x3f803f80u, 0x3f803f80u, 0x3f803f80u, 0x3f803f80u};
    {
      const u32 g = lane >> 4, hd = (w >> 1) & 1;
      u32 ra[2];
#pragma unroll
      for (int t = 0; t < 2; ++t) {
        u32 row = 8 * g + 4 * t + q4;
        u32 sw = ((row >> 1) & 1) | ((((row >> 2) ^ (row >> 3)) & 1) << 1);
        ra[t] = IMG + (w >> 2) * 24576 + row * 64 + (((p4 >> 1) ^ sw) << 4) + (p4 & 1) * 8;
      }
      const u32 oa = hd * 2048, xa = (w & 1) << 5, xb = hd << 5;
      f32x4 kv = {0.f, 0.f, 0.f, 0.f}, zz = kv;
#pragma unroll
      for (int t4 = 0; t4 < 4; ++t4) {
        v4u af = trfrag((ra[0] ^ xa) + oa + t4 * 6144, (ra[1] ^ xa) + oa + t4 * 6144);
        v4u vf = trfrag((ra[0] ^ xb) + 4096 + t4 * 6144, (ra[1] ^ xb) + 4096 + t4 * 6144);
        kv = mfma16(af, vf, kv); zz = mfma16(af, ones, zz);
      }
      st64_sc1(v2u{pk(kv[0], kv[1]), pk(kv[2], kv[3])}, rsWb, WS_KVR + ((hg * 8 + w) * 16 + rt) * 512 + lane * 8);
      if ((lane & 15) == 0) st64_sc1(v2u{pk(zz[0], zz[1]), pk(zz[2], zz[3])}, rsWb, WS_KVZ + ((hg * 8 + w) * 16 + rt) * 32 + g * 8);
    }
    STAMP(10);
    DRAIN_BAR();
    if (w == 0) publish(rsWb, F2 + (hg * 16 + rt) * 8, tlo, thi, lane);
    STAMP(3);
    v4u pq[2][2];
    {
      f32x16 pqq = logits(acc[0]);
      dual_softmax(pqq, 0, cexp, pq[0][0], pq[0][1]);
      dual_softmax(pqq, 8, cexp, pq[1][0], pq[1][1]);
    }
    if (w == 0) poll64(rsW, WS_FLAGS + xcd * FL_REP + F2 + hg * 128 + (lane & 15) * 8, tlo, thi);
    LBAR();
    STAMP(4);
    const u32 KZ = 151552, ZR = 159744;
    {
      v4u pr[8], pz;
      ld128_sc1_new(pz, l31 * 16, rsW, WS_KVZ + (hg * 8 + w) * 512);
#pragma unroll
      for (int i = 0; i < 8; ++i) ld128_sc1_new(pr[i], lane16, rsW, WS_KVR + (hg * 8 + w) * 8192 + i * 1024);
      asm volatile("s_waitcnt vmcnt(8)" : "+v"(pz)::"memory");
      sts128(ZR + w * 512 + l31 * 16, pz);
      const u32 g = lane >> 4, za = ZR + w * 512 + (8 * (g & 1) + q4) * 32 + p4 * 8;
      const u32 on = hh ? 0u : 0x3f803f80u;
      f32x4 zs = mfma16(trfrag(za, za + 128), v4u{on, on, on, on}, f32x4{0.f, 0.f, 0.f, 0.f});
      sts64(KZ + w * 1024 + 512 + (g >> 1) * 256 + (g & 1) * 8, v2u{pk(zs[0], zs[1]), pk(zs[2], zs[3])});
      float sm[8];
      u32 klo = 0x00003f80u;
      asm("" : "+s"(klo));
#pragma unroll
      for (int j = 0; j < 8; ++j) sm[j] = 0.f;
      asm volatile("s_waitcnt vmcnt(4)" : "+v"(pr[0]), "+v"(pr[1]), "+v"(pr[2]), "+v"(pr[3])::"memory");
#pragma unroll
      for (int i = 0; i < 8; ++i) {
        if (i == 4) asm volatile("s_waitcnt vmcnt(0)" : "+v"(pr[4]), "+v"(pr[5]), "+v"(pr[6]), "+v"(pr[7])::"memory");
        u32 d0 = pr[i].x, d1 = pr[i].y, d2 = pr[i].z, d3 = pr[i].w;
        sm[0] = dot2(d0, klo, sm[0]); sm[1] = dot2(d0, 0x3f800000u, sm[1]);
        sm[2] = dot2(d1, klo, sm[2]); sm[3] = dot2(d1, 0x3f800000u, sm[3]);
        sm[4] = dot2(d2, klo, sm[4]); sm[5] = dot2(d2, 0x3f800000u, sm[5]);
        sm[6] = dot2(d3, klo, sm[6]); sm[7] = dot2(d3, 0x3f800000u, sm[7]);
      }
      float t[4];
#pragma unroll
      for (int j = 0; j < 4; ++j) {
        auto r = __builtin_amdgcn_permlane32_swap(__float_as_uint(sm[j]), __float_as_uint(sm[j + 4]), false, false);
        t[j] = __uint_as_float(r[0]) + __uint_as_float(r[1]);
      }
      sts64(KZ + w * 1024 + (2 * l31 + hh) * 8, v2u{pk(t[0], t[1]), pk(t[2], t[3])});
    }
    BAR();
    {
      const u32 ka = KZ + hp * 4096 + (lo16 ? (hh * 16 + l31) * 8 : 512 + hh * 8);
      const u32 oo = WS_O + ((rt * 4 + tg) * 32 + hg * 4 + hp * 2) * 1024 + lane16;
#pragma unroll
      for (int hd = 0; hd < 2; ++hd) {
        f32x16 o = zero16();
#pragma unroll
        for (int s = 0; s < 2; ++s) {
          v2u x = lds64(ka + (hd * 2 + s) * 1024), y = lds64(ka + (hd * 2 + s) * 1024 + 256);
          o = mfma(v4u{x.x, x.y, y.x, y.y}, pq[hd][s], o);
        }
        const f32x2 i2 = spl(__builtin_amdgcn_rcpf(o[8] + 1e-6f));
        st128_sc1(v4u{pk2(f32x2{o[0], o[1]} * i2), pk2(f32x2{o[2], o[3]} * i2), pk2(f32x2{o[4], o[5]} * i2), pk2(f32x2{o[6], o[7]} * i2)}, rsWb, oo + hd * 1024);
      }
    }
    DRAIN_BAR();
    if (w == 0) publish(rsWb, F3 + (rt * 8 + hg) * 8, tlo, thi, lane);
    STAMP(5);
  } else {
    v4u xr[2];
    v2u ep;
    {
      const u32 xo = hh * 1024 + l31 * 16;
      asm volatile("buffer_load_dwordx4 %0, %2, %3, %4 offen\n\tbuffer_load_dwordx4 %1, %2, %3, %4 offen offset:512"
                   : "=&v"(xr[0]), "=&v"(xr[1]) : "v"(xo), "s"(rsX), "s"((bid * 16 + 2 * w) * 1024) : "memory");
    }
    ld64_sc1_new(ep, 0u, rsW, WS_EPOCH);
    const u32 gown = (ft == 15 ? 43u : ((ft + 1) * 8) / 3) - g0;
    const u32 fd = w >= 6 ? 1u : 0u, gs = w - 6 * fd;
    v2u bias0, bias1;
    {
      const v4u rsB = mkrs(gb1, 1376 * 2);
      const u32 bo = (g0 * 32 + gs * 16 + 4 * hh) * 2;
      asm volatile("buffer_load_dwordx2 %0, %2, %3, 0 offen\n\tbuffer_load_dwordx2 %1, %2, %3, 0 offen offset:16" : "=&v"(bias0), "=&v"(bias1) : "v"(bo), "s"(rsB) : "memory");
    }
    asm volatile("s_waitcnt vmcnt(2)" : "+v"(xr[0]), "+v"(xr[1]), "+v"(ep)::"memory");
    STAMP(1);
    tlo = rfl(ep.x) + 1u; thi = rfl(ep.y);
    const u32 HIM = 147456;
    {
      f32x2 v[8];
#pragma unroll
      for (int j = 0; j < 4; ++j) { u32 a = xr[0][j], c = xr[1][j]; v[j] = unp(a); v[4 + j] = unp(c); }
      f32x2 s = ((v[0] + v[1]) + (v[2] + v[3])) + ((v[4] + v[5]) + (v[6] + v[7]));
      const float m = hsum32(s.x + s.y) * (1.f / 512.f);
      f32x2 q = {0.f, 0.f}, q1 = q;
#pragma unroll
      for (int j = 0; j < 8; j += 2) { v[j] -= spl(m); v[j + 1] -= spl(m); q = fma2(v[j], v[j], q); q1 = fma2(v[j + 1], v[j + 1], q1); }
      q += q1;
      const float r1 = __builtin_amdgcn_rsqf(fmaf(hsum32(q.x + q.y), 1.f / 512.f, 1e-5f));
      q = f32x2{0.f, 0.f}; q1 = q;
#pragma unroll
      for (int j = 0; j < 8; j += 2) { v[j] *= spl(r1); v[j + 1] *= spl(r1); q = fma2(v[j], v[j], q); q1 = fma2(v[j + 1], v[j + 1], q1); }
      q += q1;
      s = ((v[0] + v[1]) + (v[2] + v[3])) + ((v[4] + v[5]) + (v[6] + v[7]));
      const float m2 = hsum32(s.x + s.y) * (1.f / 512.f);
      const float v2 = fmaxf(fmaf(hsum32(q.x + q.y), 1.f / 512.f, -m2 * m2), 0.f);
      const float r2 = __builtin_amdgcn_rsqf(v2 + 1e-5f);
      const f32x2 rr = spl(r2), nb = spl(-m2 * r2);
      const u32 row = 2 * w + hh, ha = HIM + row * 1024 + ((l31 ^ ((row & 3) << 2)) << 4);
      sts128(ha, v4u{pk2(fma2(v[0], rr, nb)), pk2(fma2(v[1], rr, nb)), pk2(fma2(v[2], rr, nb)), pk2(fma2(v[3], rr, nb))});
      sts128(ha + 512, v4u{pk2(fma2(v[4], rr, nb)), pk2(fma2(v[5], rr, nb)), pk2(fma2(v[6], rr, nb)), pk2(fma2(v[7], rr, nb))});
      if (l31 == 0) st128_sc1(v4u{__float_as_uint(m), tlo, __float_as_uint(r1), tlo * 0x9E3779B1u + thi}, rsWb, WS_STATS + (bid * 16 + row) * 16);
    }
    STAMP(9);
    BAR();
#pragma unroll
    for (int i = 0; i < 2; ++i) {
      const u32 d32 = 2 * w + i;
      const u32 a = HIM + (8 * hh + q4) * 1024 + (((d32 * 4 + 2 * blk + (p4 >> 1)) ^ (q4 << 2)) << 4) + (p4 & 1) * 8;
      st128_sc1(trfrag(a, a + 4096), rsWb, WS_H + (d32 * 128 + bid) * 1024 + lane16);
    }
    STAMP(10);
    asm volatile("s_waitcnt vmcnt(0) lgkmcnt(0)\n\ts_barrier" : "+v"(bias0), "+v"(bias1)::"memory");
    if (w == 0) {
      publish(rsWb, F1 + bid * 8, tlo, thi, lane);
      STAMP(2);
      poll128(rsW, WS_FLAGS + xcd * FL_REP + F1 + lane16, tlo, thi);
    }
    LBAR();
    STAMP(3);
    v4u rsH = mkrs(ws + WS_H + ((d64 * 2 + dh) * 128 + kq * 32) * 1024, 32768);
    v4u hf[8];
#pragma unroll
    for (int i = 0; i < 8; ++i) ld128_sc1_new(hf[i], lane16, rsH, (brot + i) * 1024);
    f32x16 acc[3] = {zero16(), zero16(), zero16()};
    const u32 wa = kq * 6144 + (8 * hh + q4) * 192 + blk * 32 + p4 * 8;
    v4u wf[2][3];
    auto rd = [&](int buf, u32 a) {
#pragma unroll
      for (int g = 0; g < 3; ++g) wf[buf][g] = trfrag(a + g * 64, a + 768 + g * 64);
    };
    rd(0, wa);
    u32 sb = 0, so = brot * 1024;
#pragma unroll 1
    for (u32 r = 0; r < 4; ++r) {
      so = (so + 8192) & 32767;
      if (r == 3) rsH.z = 0;
#pragma unroll
      for (int i = 0; i < 8; ++i) {
        if (i & 1) { BAR(); sb = sb == 5 * 24576 ? 0u : sb + 24576; rd(0, sb + wa); }
        else rd(1, sb + wa + 3072);
        asm volatile("s_waitcnt vmcnt(7)" : "+v"(hf[i])::"memory");
        SCHED();
#pragma unroll
        for (int g = 0; g < 3; ++g) acc[g] = mfma(wf[i & 1][g], hf[i], acc[g]);
        ld128_sc1(hf[i], lane16, rsH, so + i * 1024);
        SCHED();
      }
    }
    STAMP(4);
    const u32 RED = 49152;
#pragma unroll
    for (int g = 0; g < 3; ++g)
#pragma unroll
      for (int qq = 0; qq < 4; ++qq)
        sts128f(RED + (w * 12 + g * 4 + qq) * 1024 + lane16, f32x4{acc[g][4 * qq], acc[g][4 * qq + 1], acc[g][4 * qq + 2], acc[g][4 * qq + 3]});
    asm volatile("s_waitcnt vmcnt(0) lgkmcnt(0)\n\ts_barrier" : "+v"(hf[0]), "+v"(hf[1]), "+v"(hf[2]), "+v"(hf[3]), "+v"(hf[4]), "+v"(hf[5]), "+v"(hf[6]), "+v"(hf[7])::"memory");
    {
      f32x4 s0 = {0.f, 0.f, 0.f, 0.f}, s1 = s0;
#pragma unroll
      for (int k = 0; k < 4; ++k) {
        u32 a = RED + ((k * 2 + fd) * 12 + gs * 2) * 1024 + lane16;
        s0 += lds128f(a); s1 += lds128f(a + 1024);
      }
      f32x2 v[4] = {f32x2{s0[0], s0[1]} + unp(bias0.x), f32x2{s0[2], s0[3]} + unp(bias0.y), f32x2{s1[0], s1[1]} + unp(bias1.x), f32x2{s1[2], s1[3]} + unp(bias1.y)};
      v4u o;
#pragma unroll
      for (int j = 0; j < 4; ++j) {
        f32x2 t = v[j] * fma2(v[j] * v[j], spl(-2.f * 0.7978845608028654f * 0.044715f * LOG2E), spl(-2.f * 0.7978845608028654f * LOG2E));
        f32x2 d = f32x2{__builtin_amdgcn_exp2f(t.x), __builtin_amdgcn_exp2f(t.y)} + spl(1.f);
        o[j] = pk2(v[j] * f32x2{__builtin_amdgcn_rcpf(d.x), __builtin_amdgcn_rcpf(d.y)});
      }
      if ((gs >> 1) < gown) st128_sc1(o, rsWb, WS_U + ((d64 * 2 + fd) * 86 + g0 * 2 + gs) * 1024 + lane16);
    }
    DRAIN_BAR();
    if (w == 0) publish(rsWb, F4 + (d64 * 16 + ft) * 8, tlo, thi, lane);
    STAMP(5);
  }

  const bool isWO = kq == 3;
  const u32 etok = tid >> 3, ec = tid & 7;
  v4u xres;
  u32 b2r;
  ld128_new(xres, ((tt * 64 + etok) * 512 + dt * 64 + ec * 8) * 2, rsX, 0u);
  {
    const v4u rsB2 = mkrs(gb2, 4096);
    asm volatile("buffer_load_ushort %0, %1, %2, 0 offen" : "=v"(b2r) : "v"((tt * 64 + etok) * 2), "s"(rsB2) : "memory");
  }
  if (w == 0) {
    poll64(rsW, WS_FLAGS + xcd * FL_REP + (lane < 16 ? F4 + dt * 128 + lane * 8 : F3 + (tt >> 1) * 64 + (lane & 7) * 8), tlo, thi);
    if (b == 0 && lane == 0) st64_sc1(v2u{tlo, thi}, rsWb, WS_EPOCH);
  }
  LBAR();
  STAMP(6);
  v4u rsA = isWO ? mkrs(ws + WS_O + (tt * 2 + dh) * 32768, 32768) : mkrs(ws + WS_U + ((dt * 2 + dh) * 86 + kq * 29) * 1024, kq == 2 ? 28 * 1024 : 29 * 1024);
  v4u af[8];
#pragma unroll
  for (int i = 0; i < 8; ++i) ld128_sc1_new(af[i], lane16, rsA, (frot + i) * 1024);
  f32x16 acc[2] = {zero16(), zero16()};
  const u32 fa = kq * 4096 + (4 * hh + q4) * 128 + ((q4 >> 1) << 6) + blk * 32 + p4 * 8;
  v4u wf[2][2];
  auto rd = [&](int buf, u32 a) {
    wf[buf][0] = trfrag(a, a + 1024);
    wf[buf][1] = trfrag(a ^ 64, (a ^ 64) + 1024);
  };
  rd(0, fa);
  u32 sb = 0, so = frot * 1024;
#pragma unroll 1
  for (u32 r = 0; r < 4; ++r) {
    so = (so + 8192) & 32767;
    if (r == 3) rsA.z = 0;
#pragma unroll
    for (int i = 0; i < 8; ++i) {
      if (i & 1) { BAR(); sb = sb == 8 * 16384 ? 0u : sb + 16384; rd(0, sb + fa); }
      else rd(1, sb + fa + 2048);
      asm volatile("s_waitcnt vmcnt(7)" : "+v"(af[i])::"memory");
      SCHED();
      acc[0] = mfma(af[i], wf[i & 1][0], acc[0]);
      acc[1] = mfma(af[i], wf[i & 1][1], acc[1]);
      ld128_sc1(af[i], lane16, rsA, so + i * 1024);
      SCHED();
    }
  }
  STAMP(7);
  if (isWO) {
#pragma unroll
    for (int i = 0; i < 2; ++i)
#pragma unroll
      for (int j = 0; j < 16; ++j) {
        const u32 tk = (j & 3) + 8 * (j >> 2);
        sts32f(49152 + (dh * 32 + 4 * hh + tk) * 256 + i * 128 + ((((l31 >> 2) ^ (4 * hh + (j & 3))) & 7) << 4) + (l31 & 3) * 4, acc[i][j]);
      }
  } else {
    const u32 pb = kq * 16384 + l31 * 256 + dh * 128;
#pragma unroll
    for (int i = 0; i < 2; ++i)
#pragma unroll
      for (int qq = 0; qq < 4; ++qq)
        sts128f(pb + i * 8192 + (((2 * qq + hh) ^ (l31 & 7)) << 4), f32x4{acc[i][4 * qq], acc[i][4 * qq + 1], acc[i][4 * qq + 2], acc[i][4 * qq + 3]});
  }
  asm volatile("s_waitcnt vmcnt(0) lgkmcnt(0)\n\ts_barrier"
               : "+v"(af[0]), "+v"(af[1]), "+v"(af[2]), "+v"(af[3]), "+v"(af[4]), "+v"(af[5]), "+v"(af[6]), "+v"(af[7]), "+v"(xres), "+v"(b2r)::"memory");
  {
    const u32 a0 = etok * 256 + (((2 * ec) ^ (etok & 7)) << 4), a1 = a0 ^ 16;
    const float b2v = blo(b2r);
    f32x4 y0 = lds128f(a0) + lds128f(a0 + 16384) + lds128f(a0 + 32768);
    f32x4 y1 = lds128f(a1) + lds128f(a1 + 16384) + lds128f(a1 + 32768);
    f32x4 s0 = lds128f(a0 + 49152), s1 = lds128f(a1 + 49152);
    f32x2 y[4] = {{y0[0], y0[1]}, {y0[2], y0[3]}, {y1[0], y1[1]}, {y1[2], y1[3]}};
    f32x2 s[4] = {{s0[0], s0[1]}, {s0[2], s0[3]}, {s1[0], s1[1]}, {s1[2], s1[3]}};
    v4u o;
#pragma unroll
    for (int j = 0; j < 4; ++j) {
      f32x2 yy = y[j] + spl(b2v), t = yy * spl(-LOG2E);
      f32x2 d = f32x2{__builtin_amdgcn_exp2f(t.x), __builtin_amdgcn_exp2f(t.y)} + spl(1.f);
      f32x2 g = yy * f32x2{__builtin_amdgcn_rcpf(d.x), __builtin_amdgcn_rcpf(d.y)};
      u32 xd = xres[j];
      o[j] = pk2(fma2(s[j], g, unp(xd)));
    }
    *(v4u*)(gout + (tt * 64 + etok) * 512 + dt * 64 + ec * 8) = o;
  }
}

extern "C" void kernel_launch(void* const* d_in, const int* in_sizes, int n_in, void* d_out, int out_size, void* d_ws, size_t ws_size,
                              hipStream_t stream) {
  (void)in_sizes; (void)n_in; (void)out_size; (void)ws_size;
  (void)hipFuncSetAttribute((const void*)fused_kernel, hipFuncAttributeMaxDynamicSharedMemorySize, LDS_BYTES);
  fused_kernel<<<256, 768, LDS_BYTES, stream>>>((const u16*)d_in[0], (const u16*)d_in[1], (const u16*)d_in[2], (const u16*)d_in[3],
                                                (const u16*)d_in[5], (const u16*)d_in[6], (const u16*)d_in[7], (const u16*)d_in[8],
                                                (u16*)d_out, (char*)d_ws);
}
```

```cpp
#define STAMPS 1
#include <hip/hip_runtime.h>
#include <stdint.h>

typedef unsigned u32;
typedef unsigned short u16;
typedef unsigned long long u64;
typedef u32 v4u __attribute__((ext_vector_type(4)));
typedef u32 v2u __attribute__((ext_vector_type(2)));
typedef float f32x16 __attribute__((ext_vector_type(16)));
typedef float f32x4 __attribute__((ext_vector_type(4)));
typedef float f32x2 __attribute__((ext_vector_type(2)));
typedef short s16x4 __attribute__((ext_vector_type(4)));
typedef __bf16 bf16x8 __attribute__((ext_vector_type(8)));
typedef __bf16 bf16x2 __attribute__((ext_vector_type(2)));
#define LAS __attribute__((address_space(3)))
#define DI __device__ __forceinline__

#define HOLD 1

constexpr u32 WS_EPOCH = 0, WS_FLAGS = 4096, FL_REP = 4096;
constexpr u32 F1 = 0, F2 = 1024, F3 = 2048, F4 = 3072;
constexpr u32 WS_STATS = 65536;
constexpr u32 WS_KVZ = 131072;
constexpr u32 WS_KVR = 262144;
constexpr u32 WS_H = 1u << 20;
constexpr u32 WS_O = 3u << 20;
constexpr u32 WS_U = 5u << 20;
constexpr u32 WS_BYTES = 7u << 20;
constexpr u32 WS_STAMP = 8u << 20;
constexpr u32 LDS_BYTES = 163840;
constexpr u32 CODE_BYTES = 19 * 1024;
constexpr u32 TOUCH_W = 3;
constexpr u32 SPIN_CAP = 1u << 15;
constexpr float LOG2E = 1.4426950408889634f;

DI u32 rfl(u32 v) { return (u32)__builtin_amdgcn_readfirstlane((int)v); }
DI v4u mkrs(const void* p, u32 bytes) { u64 a = (u64)p; return v4u{(u32)a, (u32)(a >> 32) & 0xffffu, bytes, 0x00020000u}; }
DI __amdgpu_buffer_rsrc_t mkrsb(const void* p, u32 bytes) { return __builtin_amdgcn_make_buffer_rsrc((void*)p, (short)0, (int)bytes, 0x00020000); }
DI void gl4(u32 m, u32 v0, u32 v1, u32 v2, u32 v3, v4u rs, u32 sa, u32 sb) {
  asm volatile("s_mov_b32 m0, %0\n\ts_nop 0\n\t"
               "buffer_load_dwordx4 %1, %5, %6 offen lds\n\tbuffer_load_dwordx4 %2, %5, %6 offen offset:1024 lds\n\t"
               "buffer_load_dwordx4 %3, %5, %7 offen offset:2048 lds\n\tbuffer_load_dwordx4 %4, %5, %7 offen offset:3072 lds"
               ::"s"(m), "v"(v0), "v"(v1), "v"(v2), "v"(v3), "s"(rs), "s"(sa), "s"(sb) : "memory");
}
DI void gl3(u32 m, u32 v0, u32 v1, u32 v2, v4u rs, u32 sa) {
  asm volatile("s_mov_b32 m0, %0\n\ts_nop 0\n\t"
               "buffer_load_dwordx4 %1, %4, %5 offen lds\n\tbuffer_load_dwordx4 %2, %4, %5 offen offset:1024 lds\n\t"
               "buffer_load_dwordx4 %3, %4, %5 offen offset:2048 lds"
               ::"s"(m), "v"(v0), "v"(v1), "v"(v2), "s"(rs), "s"(sa) : "memory");
}
DI void ld128_sc1(v4u& d, u32 voff, v4u rs, u32 soff) {
  asm volatile("buffer_load_dwordx4 %0, %1, %2, %3 offen sc1" : "+v"(d) : "v"(voff), "s"(rs), "s"(soff) : "memory");
}
DI void ld128_sc1_new(v4u& d, u32 voff, v4u rs, u32 soff) {
  asm volatile("buffer_load_dwordx4 %0, %1, %2, %3 offen sc1" : "=v"(d) : "v"(voff), "s"(rs), "s"(soff) : "memory");
}
DI void ld128_new(v4u& d, u32 voff, v4u rs, u32 soff) {
  asm volatile("buffer_load_dwordx4 %0, %1, %2, %3 offen" : "=v"(d) : "v"(voff), "s"(rs), "s"(soff) : "memory");
}
DI void ld64_sc1_new(v2u& d, u32 voff, v4u rs, u32 soff) {
  asm volatile("buffer_load_dwordx2 %0, %1, %2, %3 offen sc1" : "=v"(d) : "v"(voff), "s"(rs), "s"(soff) : "memory");
}
DI void st128_sc1(v4u v, __amdgpu_buffer_rsrc_t rs, u32 off) { __builtin_amdgcn_raw_buffer_store_b128(v, rs, (int)off, 0, 16); }
DI void st64_sc1(v2u v, __amdgpu_buffer_rsrc_t rs, u32 off) { __builtin_amdgcn_raw_buffer_store_b64(v, rs, (int)off, 0, 16); }
#define BAR() asm volatile("s_waitcnt lgkmcnt(0)\n\ts_barrier" ::: "memory")
#define DRAIN_BAR() asm volatile("s_waitcnt vmcnt(0) lgkmcnt(0)\n\ts_barrier" ::: "memory")
#define VBAR(n) asm volatile("s_waitcnt vmcnt(" #n ")\n\ts_barrier" ::: "memory")
#define LBAR() asm volatile("s_barrier" ::: "memory")
#define SCHED() __builtin_amdgcn_sched_barrier(0)

DI v2u trrd(u32 a) { return __builtin_bit_cast(v2u, __builtin_amdgcn_ds_read_tr16_b64_v4i16((LAS s16x4*)(size_t)a)); }
DI v4u trfrag(u32 a0, u32 a1) { v2u x = trrd(a0), y = trrd(a1); return v4u{x.x, x.y, y.x, y.y}; }
DI v4u lds128(u32 a) { return *(LAS v4u*)(size_t)a; }
DI v2u lds64(u32 a) { return *(LAS v2u*)(size_t)a; }
DI f32x4 lds128f(u32 a) { return *(LAS f32x4*)(size_t)a; }
DI void sts128(u32 a, v4u v) { *(LAS v4u*)(size_t)a = v; }
DI void sts128f(u32 a, f32x4 v) { *(LAS f32x4*)(size_t)a = v; }
DI void sts64(u32 a, v2u v) { *(LAS v2u*)(size_t)a = v; }
DI void sts32f(u32 a, float v) { *(LAS float*)(size_t)a = v; }
DI f32x16 mfma(v4u a, v4u b, f32x16 c) {
  return __builtin_amdgcn_mfma_f32_32x32x16_bf16(__builtin_bit_cast(bf16x8, a), __builtin_bit_cast(bf16x8, b), c, 0, 0, 0);
}
DI f32x4 mfma16(v4u a, v4u b, f32x4 c) {
  return __builtin_amdgcn_mfma_f32_16x16x32_bf16(__builtin_bit_cast(bf16x8, a), __builtin_bit_cast(bf16x8, b), c, 0, 0, 0);
}
DI u32 pk2(f32x2 v) { return __builtin_bit_cast(u32, __builtin_convertvector(v, bf16x2)); }
DI u32 pk(float a, float b) { return pk2(f32x2{a, b}); }
DI float blo(u32 v) { return __uint_as_float(v << 16); }
DI float bhi(u32 v) { return __uint_as_float(v & 0xffff0000u); }
DI f32x2 unp(u32 v) { return f32x2{blo(v), bhi(v)}; }
DI f32x2 fma2(f32x2 a, f32x2 b, f32x2 c) { return __builtin_elementwise_fma(a, b, c); }
DI f32x2 spl(float a) { return f32x2{a, a}; }
DI float dot2(u32 a, u32 b, float c) { return __builtin_amdgcn_fdot2_f32_bf16(__builtin_bit_cast(bf16x2, a), __builtin_bit_cast(bf16x2, b), c, false); }
template <int C> DI float dpp(float v) { return __int_as_float(__builtin_amdgcn_mov_dpp(__float_as_int(v), C, 0xF, 0xF, true)); }
DI float x16sum(float v) { u32 x = __float_as_uint(v); auto r = __builtin_amdgcn_permlane16_swap(x, x, false, false); return __uint_as_float(r[0]) + __uint_as_float(r[1]); }
DI float x32sum(float v) { u32 x = __float_as_uint(v); auto r = __builtin_amdgcn_permlane32_swap(x, x, false, false); return __uint_as_float(r[0]) + __uint_as_float(r[1]); }
DI float x32max(float v) { u32 x = __float_as_uint(v); auto r = __builtin_amdgcn_permlane32_swap(x, x, false, false); return fmaxf(__uint_as_float(r[0]), __uint_as_float(r[1])); }
DI float x32min(float v) { u32 x = __float_as_uint(v); auto r = __builtin_amdgcn_permlane32_swap(x, x, false, false); return fminf(__uint_as_float(r[0]), __uint_as_float(r[1])); }
DI float hsum32(float v) {
  v += dpp<0xB1>(v); v += dpp<0x4E>(v); v += dpp<0x141>(v); v += dpp<0x140>(v);
  return x16sum(v);
}
DI f32x16 zero16() { f32x16 z;
#pragma unroll
  for (int j = 0; j < 16; ++j) z[j] = 0.f;
  return z; }

DI void publish(__amdgpu_buffer_rsrc_t rsW, u32 off, u32 tlo, u32 thi, u32 lane) {
  if (lane < 8) st64_sc1(v2u{tlo, thi}, rsW, WS_FLAGS + lane * FL_REP + off);
}
DI void poll64(v4u rsW, u32 off, u32 tlo, u32 thi) {
#pragma unroll 1
  for (u32 spin = 0; spin < SPIN_CAP; ++spin) {
    v2u t;
    asm volatile("buffer_load_dwordx2 %0, %1, %2, 0 offen sc1\n\ts_waitcnt vmcnt(0)" : "=&v"(t) : "v"(off), "s"(rsW) : "memory");
    if (__all(t.x == tlo && t.y == thi)) break;
  }
}
DI void poll128(v4u rsW, u32 off, u32 tlo, u32 thi) {
#pragma unroll 1
  for (u32 spin = 0; spin < SPIN_CAP; ++spin) {
    v4u t;
    asm volatile("buffer_load_dwordx4 %0, %1, %2, 0 offen sc1\n\ts_waitcnt vmcnt(0)" : "=&v"(t) : "v"(off), "s"(rsW) : "memory");
    if (__all(t.x == tlo && t.y == thi && t.z == tlo && t.w == thi)) break;
  }
}

DI void dual_softmax(const f32x16& z, int o, float c, v4u& fp, v4u& fn) {
  f32x2 p[4] = {{z[o], z[o + 1]}, {z[o + 2], z[o + 3]}, {z[o + 4], z[o + 5]}, {z[o + 6], z[o + 7]}};
  float mx = fmaxf(fmaxf(fmaxf(p[0].x, p[0].y), p[1].x), fmaxf(fmaxf(p[1].y, p[2].x), p[2].y));
  mx = fmaxf(fmaxf(mx, p[3].x), p[3].y);
  float mn = fminf(fminf(fminf(p[0].x, p[0].y), p[1].x), fminf(fminf(p[1].y, p[2].x), p[2].y));
  mn = fminf(fminf(mn, p[3].x), p[3].y);
  mx = x32max(mx); mn = x32min(mn);
  const f32x2 a = spl(-mx * c), b = spl(mn * c), c2 = spl(c), n2 = spl(-c);
  f32x2 e[4], g[4];
#pragma unroll
  for (int i = 0; i < 4; ++i) {
    f32x2 t = fma2(p[i], c2, a), u = fma2(p[i], n2, b);
    e[i] = f32x2{__builtin_amdgcn_exp2f(t.x), __builtin_amdgcn_exp2f(t.y)};
    g[i] = f32x2{__builtin_amdgcn_exp2f(u.x), __builtin_amdgcn_exp2f(u.y)};
  }
  f32x2 se = (e[0] + e[1]) + (e[2] + e[3]), sg = (g[0] + g[1]) + (g[2] + g[3]);
  const f32x2 ie = spl(__builtin_amdgcn_rcpf(x32sum(se.x + se.y))), ig = spl(__builtin_amdgcn_rcpf(x32sum(sg.x + sg.y)));
  fp = v4u{pk2(e[0] * ie), pk2(e[1] * ie), pk2(e[2] * ie), pk2(e[3] * ie)};
  fn = v4u{pk2(g[0] * ig), pk2(g[1] * ig), pk2(g[2] * ig), pk2(g[3] * ig)};
}

#define STAMP(i) do { stamp[i] = __builtin_amdgcn_s_memrealtime(); cst[i] = (u32)__builtin_amdgcn_s_memtime(); } while (0)

#define TS(base, n) do {} while (0)
#define TSDUMP(base, sec) do {} while (0)
#define VBART(n, base, k) VBAR(n)
__global__ __launch_bounds__(768, 3) void fused_kernel(const u16* __restrict__ gx, const u16* __restrict__ gwqkv, const u16* __restrict__ gwphi,
                                                         const u16* __restrict__ gwo, const u16* __restrict__ gw1, const u16* __restrict__ gb1,
                                                         const u16* __restrict__ gw2, const u16* __restrict__ gb2, u16* __restrict__ gout, char* ws) {
  u64 stamp[12]; u32 cst[12];
#pragma unroll
  for (int i = 0; i < 12; ++i) { stamp[i] = 0; cst[i] = 0; }
  u64 cyc0 = __builtin_amdgcn_s_memtime();
  u64 pc0;
  asm volatile("s_getpc_b64 %0" : "=s"(pc0));
  STAMP(0);
  const u32 tid = threadIdx.x, lane = tid & 63;
  const u32 w = rfl(tid >> 6);
  const u32 b = blockIdx.x, xcd = b & 7, idx = b >> 3, li = idx >> 1;
  const bool roleA = (idx & 1) == 0;
  const u32 lane16 = lane * 16;
  const u32 tt = xcd * 4 + (idx >> 3), dt = idx & 7;
  const u32 frot = (((idx >> 3) + (dt >> 1)) * 8) & 31;
  const u32 rt = (xcd >> 1) * 4 + (li >> 2), hg = (xcd & 1) * 4 + (li & 3);
  const u32 bid = xcd * 16 + li;
  const u32 d64 = (xcd & 1) * 4 + (li & 3), ft = (xcd >> 1) * 4 + (li >> 2);
  const u32 g0 = (ft * 8) / 3;
  const u32 brot = (((li & 3) + (li >> 2)) * 8) & 31;

  if (w >= 8) {
    const u32 lw = w - 8;
    const v4u rsC = mkrs((const void*)(pc0 & ~(u64)255), CODE_BYTES);
    const u32 cvo = (li * 16 + lw * 4 + (lane & 3)) * 128 + (lane >> 2) * (64 * 1024);
    auto touch = [&](u32 la) {
      asm volatile("s_mov_b32 m0, %0\n\ts_nop 0\n\tbuffer_load_dword %1, %2, 0 offen lds" ::"s"(la), "v"(cvo), "s"(rsC) : "memory");
    };
    touch(roleA ? 150528u : 5 * 24576 + lw * 6144);
    const bool wo = lw == 3;
    const u32 fpitch = wo ? 1024u : 4096u, fsh = wo ? 14u : 16u;
    const u32 fcol = wo ? dt * 128 : tt * 128, fcnt = wo ? 32u : (lw == 2 ? 28u : 29u);
    const char* fbase = wo ? (const char*)gwo : (const char*)gw2 + (size_t)(lw * 29) * 65536;
    const v4u rsF = mkrs(fbase + fcol - 4096, (fcnt << fsh) - fcol + 4096);
    const u32 fv0 = (lane >> 3) * fpitch + (((lane & 7) ^ (((lane >> 4) & 1) << 2)) << 4) + 4096;
    const u32 fv1 = fv0 + 8 * fpitch - 1024, fv2 = fv0 - 2048, fv3 = fv1 - 2048;
    auto fin = [&](u32 st, u32 slot) {
      u32 i = (2 * st + frot) & 31;
      gl4(slot * 16384 + lw * 4096, fv0, fv1, fv2, fv3, rsF, i << fsh, (i + 1) << fsh);
    };
    if (roleA) {
      const v4u rsX = mkrs((const char*)gx - 4096, 2048 * 1024 + 4096), rsQ = mkrs((const char*)gwqkv - 4096, 512 * 3072 + 4096);
      const u32 rot = (2 * ((li >> 2) + (li & 3))) & 7;
      u32 xv[4], wv[6];
#pragma unroll
      for (int i = 0; i < 4; ++i) {
        u32 row = 32 * lw + 8 * i + (lane >> 3);
        xv[i] = (rt * 128 + row) * 1024 + (((lane & 7) ^ ((row >> 1) & 7)) << 4) + 4096 - i * 1024;
      }
#pragma unroll
      for (int i = 0; i < 3; ++i) {
        u32 o = i * 1024 + lane16, row = o / 384, c = (o - row * 384) >> 4;
        wv[i] = (16 * lw + row) * 3072 + (c >> 3) * 1024 + hg * 128 + (((c & 7) ^ (((row >> 1) & 1) << 2)) << 4) + 4096 - i * 1024;
        wv[3 + i] = wv[i] + 8 * 3072;
      }
      auto stage = [&](u32 st, u32 slot) {
        u32 k = (st + rot) & 7, la = slot * 40960;
        gl4(la + lw * 4096, xv[0], xv[1], xv[2], xv[3], rsX, k * 128, k * 128);
        gl3(la + 16384 + lw * 6144, wv[0], wv[1], wv[2], rsQ, k * (64 * 3072));
        gl3(la + 16384 + lw * 6144 + 3072, wv[3], wv[4], wv[5], rsQ, k * (64 * 3072));
      };
      stage(0, 0); stage(1, 1); stage(2, 2);
      VBAR(20);
      u32 slot = 0;
#pragma unroll 1
      for (u32 j = 0; j < 5; ++j) { VBART(10, 131072, j); stage(j + 3, slot); TS(131072, 3 * j + 2); slot = slot == 2 ? 0u : slot + 1; }
      touch(150528u);
      VBART(11, 131072, 5); VBART(1, 131072, 6); LBAR(); TS(131072, 22); TSDUMP(131072, 0);
      fin(0, 0); fin(1, 1);
      LBAR(); LBAR(); LBAR();
      touch(150528u);
      fin(2, 2); fin(3, 3);
      LBAR(); LBAR();
      fin(4, 4); fin(5, 5); fin(6, 6); fin(7, 7); fin(8, 8);
      LBAR();
    } else {
      const v4u rs1 = mkrs((const char*)gw1 + (size_t)(lw * 512) * 2752 + g0 * 64 - 4096, 512 * 2752 - g0 * 64 + 4096);
      u32 wv[3];
#pragma unroll
      for (int i = 0; i < 3; ++i) { u32 o = i * 1024 + lane16, row = o / 192; wv[i] = row * 2752 + (o - row * 192) + 4096 - i * 1024; }
      auto stage = [&](u32 st, u32 slot) {
        u32 so = ((2 * st + brot) & 31) * (16 * 2752), la = slot * 24576 + lw * 6144;
        gl3(la, wv[0], wv[1], wv[2], rs1, so);
        gl3(la + 3072, wv[0], wv[1], wv[2], rs1, so + 16 * 2752);
      };
      LBAR();
      LBAR();
      stage(0, 0); stage(1, 1); stage(2, 2); stage(3, 3); stage(4, 4); stage(5, 5);
      VBAR(30);
      u32 slot = 0;
#pragma unroll 1
      for (u32 j = 0; j < 10; ++j) { VBART(24, 155648, j); stage(j + 6, slot); TS(155648, 3 * j + 2); slot = slot == 5 ? 0u : slot + 1; }
      touch(150528u);
      VBART(25, 155648, 10); VBART(19, 155648, 11); VBART(13, 155648, 12); VBART(7, 155648, 13);
      fin(0, 0); fin(1, 1); fin(2, 2);
      VBART(13, 155648, 14); LBAR(); TS(155648, 46); TSDUMP(155648, 0);
      LBAR();
      {
        const __amdgpu_buffer_rsrc_t rsWb = mkrsb(ws, WS_BYTES);
        const u32 gown = (ft == 15 ? 43u : ((ft + 1) * 8) / 3) - g0;
        const u32 gs = 2 + lw, hh = lane >> 5;
        const u16* p = gb1 + g0 * 32 + gs * 16 + 4 * hh;
        const v2u b0 = *(const v2u*)p, b1 = *(const v2u*)(p + 8);
        f32x4 s0 = {0.f, 0.f, 0.f, 0.f}, s1 = s0;
#pragma unroll
        for (int k = 0; k < 4; ++k) {
          u32 a = 49152 + ((k * 2 + 1) * 12 + gs * 2) * 1024 + lane16;
          s0 += lds128f(a); s1 += lds128f(a + 1024);
        }
        f32x2 v[4] = {f32x2{s0[0], s0[1]} + unp(b0.x), f32x2{s0[2], s0[3]} + unp(b0.y), f32x2{s1[0], s1[1]} + unp(b1.x), f32x2{s1[2], s1[3]} + unp(b1.y)};
        v4u o;
#pragma unroll
        for (int j = 0; j < 4; ++j) {
          f32x2 t = v[j] * fma2(v[j] * v[j], spl(-2.f * 0.7978845608028654f * 0.044715f * LOG2E), spl(-2.f * 0.7978845608028654f * LOG2E));
          f32x2 d = f32x2{__builtin_amdgcn_exp2f(t.x), __builtin_amdgcn_exp2f(t.y)} + spl(1.f);
          o[j] = pk2(v[j] * f32x2{__builtin_amdgcn_rcpf(d.x), __builtin_amdgcn_rcpf(d.y)});
        }
        if ((gs >> 1) < gown) st128_sc1(o, rsWb, WS_U + ((d64 * 2 + 1) * 86 + g0 * 2 + gs) * 1024 + lane16);
      }
      DRAIN_BAR();
      fin(3, 3); fin(4, 4); fin(5, 5); fin(6, 6); fin(7, 7); fin(8, 8);
    }
    VBAR(32);
#pragma unroll 1
    for (u32 j = 0; j < 7; ++j) { VBART(28, 155648, j); fin(j + 9, j); TS(155648, 3 * j + 2); }
    touch(150528u);
    VBART(29, 155648, 7); VBART(25, 155648, 8); VBART(21, 155648, 9); VBART(17, 155648, 10); VBART(13, 155648, 11); VBART(9, 155648, 12); VBART(5, 155648, 13); VBART(1, 155648, 14);
    LBAR(); TS(155648, 46); TSDUMP(155648, 1); LBAR();
    return;
  }

  const u32 l31 = lane & 31, hh = lane >> 5, blk = (lane >> 4) & 1, q4 = (lane & 15) >> 2, p4 = lane & 3;
  const v4u rsW = mkrs(ws, WS_BYTES);
  const __amdgpu_buffer_rsrc_t rsWb = mkrsb(ws, WS_BYTES);
  const v4u rsX = mkrs(gx, 2048 * 1024);
  const u32 dh = w & 1, kq = w >> 1;
  u32 tlo, thi;

  if (roleA) {
    v2u ep;
    ld64_sc1_new(ep, 0u, rsW, WS_EPOCH);
    const u32 tg = w & 3, hp = w >> 2;
    v4u wph;
    {
      const u16* p = gwphi + (hg * 4 + hp * 2 + (l31 >> 4)) * 256 + (4 * hh) * 16 + (l31 & 15);
      wph.x = p[0] | ((u32)p[16] << 16); wph.y = p[32] | ((u32)p[48] << 16);
      wph.z = p[128] | ((u32)p[144] << 16); wph.w = p[160] | ((u32)p[176] << 16);
    }
    const u32 xa0 = (32 * tg + l31) * 128 + ((hh ^ ((l31 >> 1) & 7)) << 4);
    const u32 wa = 16384 + (8 * hh + q4) * 384 + ((hp ^ (q4 >> 1)) << 6) + blk * 32 + p4 * 8;
    f32x16 acc[3] = {zero16(), zero16(), zero16()};
    float cs[3] = {0.f, 0.f, 0.f};
    v4u stg = {0u, 0u, 0u, 0u};
    const u32 stoff = WS_STATS + (rt * 128 + 32 * tg + l31) * 16;
    v4u xf[2], wf[2][3];
    auto rd = [&](int buf, u32 sb, int ks) {
      xf[buf] = lds128(sb + (xa0 ^ (ks << 5)));
#pragma unroll
      for (int s = 0; s < 3; ++s) wf[buf][s] = trfrag(sb + wa + ks * 6144 + s * 128, sb + wa + ks * 6144 + 1536 + s * 128);
    };
    BAR();
    STAMP(1);
    rd(0, 0, 0);
    u32 sb = 0;
#pragma unroll 1
    for (u32 st = 0; st < 8; ++st) {
#pragma unroll
      for (int ks = 0; ks < 4; ++ks) {
        const int c = ks & 1;
        if (ks == 3) { BAR(); sb = sb == 81920 ? 0u : sb + 40960; rd(c ^ 1, sb, 0); }
        else rd(c ^ 1, sb, ks + 1);
        SCHED();
#pragma unroll
        for (int s = 0; s < 3; ++s) acc[s] = mfma(wf[c][s], xf[c], acc[s]);
        if (tg == (u32)ks) {
#pragma unroll
          for (int s = 0; s < 3; ++s) {
            u32 a0 = wf[c][s].x, a1 = wf[c][s].y, a2 = wf[c][s].z, a3 = wf[c][s].w;
            cs[s] = dot2(a0, 0x3f803f80u, cs[s]); cs[s] = dot2(a1, 0x3f803f80u, cs[s]);
            cs[s] = dot2(a2, 0x3f803f80u, cs[s]); cs[s] = dot2(a3, 0x3f803f80u, cs[s]);
          }
        }
        if (ks == 0 && st == 7) ld128_sc1(stg, stoff, rsW, 0u);
        SCHED();
      }
    }
    STAMP(2);
    const u32 CSP = 147456;
#pragma unroll
    for (int s = 0; s < 3; ++s) sts32f(CSP + (hp * 3 + s) * 512 + l31 * 16 + tg * 4, x32sum(cs[s]));
    asm volatile("s_waitcnt vmcnt(0) lgkmcnt(0)\n\ts_barrier" : "+v"(stg), "+v"(ep)::"memory");
    tlo = rfl(ep.x) + 1u; thi = rfl(ep.y);
    const u32 tB = tlo * 0x9E3779B1u + thi;
    v4u ca[3];
#pragma unroll
    for (int s = 0; s < 3; ++s) {
      f32x4 c4 = lds128f(CSP + (hp * 3 + s) * 512 + l31 * 16);
      float c = (c4[0] + c4[1]) + (c4[2] + c4[3]);
      u32 hi = pk(c, c);
      ca[s] = v4u{pk(c, c - blo(hi)), hi, 0u, 0u};
    }
#pragma unroll 1
    for (u32 spin = 0; spin < SPIN_CAP; ++spin) {
      if (__all(stg.y == tlo && stg.w == tB)) break;
      asm volatile("buffer_load_dwordx4 %0, %1, %2, 0 offen sc1\n\ts_waitcnt vmcnt(0)" : "+v"(stg) : "v"(stoff), "s"(rsW) : "memory");
    }
    const float rstd = __uint_as_float(stg.z), cexp = rstd * LOG2E;
    {
      const float nm = hh ? 0.f : -__uint_as_float(stg.x);
      const u32 mh = pk(nm, nm);
      const v4u mb = {mh, pk(nm - blo(mh), 0.f), 0u, 0u};
      acc[1] = mfma(ca[1], mb, acc[1]); acc[2] = mfma(ca[2], mb, acc[2]); acc[0] = mfma(ca[0], mb, acc[0]);
    }
    const bool lo16 = l31 < 16;
    const v4u wph0 = {lo16 ? wph.x : 0u, lo16 ? wph.y : 0u, lo16 ? wph.z : 0u, lo16 ? wph.w : 0u};
    const v4u wph1 = {wph.x ^ wph0.x, wph.y ^ wph0.y, wph.z ^ wph0.z, wph.w ^ wph0.w};
    auto logits = [&](const f32x16& a) {
      v4u f0 = {pk(a[0], a[1]), pk(a[2], a[3]), pk(a[4], a[5]), pk(a[6], a[7])};
      v4u f1 = {pk(a[8], a[9]), pk(a[10], a[11]), pk(a[12], a[13]), pk(a[14], a[15])};
      return mfma(wph1, f1, mfma(wph0, f0, zero16()));
    };
    const u32 IMG = 98304;
    {
      f32x16 pkk = logits(acc[1]);
      const u32 sw = ((l31 >> 1) & 1) | ((((l31 >> 2) ^ (l31 >> 3)) & 1) << 1);
      const u32 wa2 = IMG + w * 6144 + l31 * 64 + hh * 8 + (sw << 4);
      const f32x2 r2 = spl(rstd);
#pragma unroll
      for (int qq = 0; qq < 4; ++qq)
        sts64((wa2 ^ (qq << 4)) + 4096, v2u{pk2(f32x2{acc[2][4 * qq], acc[2][4 * qq + 1]} * r2), pk2(f32x2{acc[2][4 * qq + 2], acc[2][4 * qq + 3]} * r2)});
#pragma unroll
      for (int hd = 0; hd < 2; ++hd) {
        v4u fp, fn;
        dual_softmax(pkk, 8 * hd, cexp, fp, fn);
        sts64((wa2 ^ 0) + hd * 2048, v2u{fp.x, fp.y});
        sts64((wa2 ^ 16) + hd * 2048, v2u{fp.z, fp.w});
        sts64((wa2 ^ 32) + hd * 2048, v2u{fn.x, fn.y});
        sts64((wa2 ^ 48) + hd * 2048, v2u{fn.z, fn.w});
      }
    }
    STAMP(9);
    BAR();
    const v4u ones = {0x3f803f80u, 0x3f803f80u, 0x3f803f80u, 0x3f803f80u};
    {
      const u32 g = lane >> 4, hd = (w >> 1) & 1;
      u32 ra[2];
#pragma unroll
      for (int t = 0; t < 2; ++t) {
        u32 row = 8 * g + 4 * t + q4;
        u32 sw = ((row >> 1) & 1) | ((((row >> 2) ^ (row >> 3)) & 1) << 1);
        ra[t] = IMG + (w >> 2) * 24576 + row * 64 + (((p4 >> 1) ^ sw) << 4) + (p4 & 1) * 8;
      }
      const u32 oa = hd * 2048, xa = (w & 1) << 5, xb = hd << 5;
      f32x4 kv = {0.f, 0.f, 0.f, 0.f}, zz = kv;
#pragma unroll
      for (int t4 = 0; t4 < 4; ++t4) {
        v4u af = trfrag((ra[0] ^ xa) + oa + t4 * 6144, (ra[1] ^ xa) + oa + t4 * 6144);
        v4u vf = trfrag((ra[0] ^ xb) + 4096 + t4 * 6144, (ra[1] ^ xb) + 4096 + t4 * 6144);
        kv = mfma16(af, vf, kv); zz = mfma16(af, ones, zz);
      }
      st64_sc1(v2u{pk(kv[0], kv[1]), pk(kv[2], kv[3])}, rsWb, WS_KVR + ((hg * 8 + w) * 16 + rt) * 512 + lane * 8);
      if ((lane & 15) == 0) st64_sc1(v2u{pk(zz[0], zz[1]), pk(zz[2], zz[3])}, rsWb, WS_KVZ + ((hg * 8 + w) * 16 + rt) * 32 + g * 8);
    }
    STAMP(10);
    DRAIN_BAR();
    if (w == 0) publish(rsWb, F2 + (hg * 16 + rt) * 8, tlo, thi, lane);
    STAMP(3);
    v4u pq[2][2];
    {
      f32x16 pqq = logits(acc[0]);
      dual_softmax(pqq, 0, cexp, pq[0][0], pq[0][1]);
      dual_softmax(pqq, 8, cexp, pq[1][0], pq[1][1]);
    }
    if (w == 0) poll64(rsW, WS_FLAGS + xcd * FL_REP + F2 + hg * 128 + (lane & 15) * 8, tlo, thi);
    LBAR();
    STAMP(4);
    const u32 KZ = 151552, ZR = 159744;
    {
      v4u pr[8], pz;
      ld128_sc1_new(pz, l31 * 16, rsW, WS_KVZ + (hg * 8 + w) * 512);
#pragma unroll
      for (int i = 0; i < 8; ++i) ld128_sc1_new(pr[i], lane16, rsW, WS_KVR + (hg * 8 + w) * 8192 + i * 1024);
      asm volatile("s_waitcnt vmcnt(8)" : "+v"(pz)::"memory");
      sts128(ZR + w * 512 + l31 * 16, pz);
      const u32 g = lane >> 4, za = ZR + w * 512 + (8 * (g & 1) + q4) * 32 + p4 * 8;
      const u32 on = hh ? 0u : 0x3f803f80u;
      f32x4 zs = mfma16(trfrag(za, za + 128), v4u{on, on, on, on}, f32x4{0.f, 0.f, 0.f, 0.f});
      sts64(KZ + w * 1024 + 512 + (g >> 1) * 256 + (g & 1) * 8, v2u{pk(zs[0], zs[1]), pk(zs[2], zs[3])});
      float sm[8];
      u32 klo = 0x00003f80u;
      asm("" : "+s"(klo));
#pragma unroll
      for (int j = 0; j < 8; ++j) sm[j] = 0.f;
      asm volatile("s_waitcnt vmcnt(4)" : "+v"(pr[0]), "+v"(pr[1]), "+v"(pr[2]), "+v"(pr[3])::"memory");
#pragma unroll
      for (int i = 0; i < 8; ++i) {
        if (i == 4) asm volatile("s_waitcnt vmcnt(0)" : "+v"(pr[4]), "+v"(pr[5]), "+v"(pr[6]), "+v"(pr[7])::"memory");
        u32 d0 = pr[i].x, d1 = pr[i].y, d2 = pr[i].z, d3 = pr[i].w;
        sm[0] = dot2(d0, klo, sm[0]); sm[1] = dot2(d0, 0x3f800000u, sm[1]);
        sm[2] = dot2(d1, klo, sm[2]); sm[3] = dot2(d1, 0x3f800000u, sm[3]);
        sm[4] = dot2(d2, klo, sm[4]); sm[5] = dot2(d2, 0x3f800000u, sm[5]);
        sm[6] = dot2(d3, klo, sm[6]); sm[7] = dot2(d3, 0x3f800000u, sm[7]);
      }
      float t[4];
#pragma unroll
      for (int j = 0; j < 4; ++j) {
        auto r = __builtin_amdgcn_permlane32_swap(__float_as_uint(sm[j]), __float_as_uint(sm[j + 4]), false, false);
        t[j] = __uint_as_float(r[0]) + __uint_as_float(r[1]);
      }
      sts64(KZ + w * 1024 + (2 * l31 + hh) * 8, v2u{pk(t[0], t[1]), pk(t[2], t[3])});
    }
    BAR();
    {
      const u32 ka = KZ + hp * 4096 + (lo16 ? (hh * 16 + l31) * 8 : 512 + hh * 8);
      const u32 oo = WS_O + ((rt * 4 + tg) * 32 + hg * 4 + hp * 2) * 1024 + lane16;
#pragma unroll
      for (int hd = 0; hd < 2; ++hd) {
        f32x16 o = zero16();
#pragma unroll
        for (int s = 0; s < 2; ++s) {
          v2u x = lds64(ka + (hd * 2 + s) * 1024), y = lds64(ka + (hd * 2 + s) * 1024 + 256);
          o = mfma(v4u{x.x, x.y, y.x, y.y}, pq[hd][s], o);
        }
        const f32x2 i2 = spl(__builtin_amdgcn_rcpf(o[8] + 1e-6f));
        st128_sc1(v4u{pk2(f32x2{o[0], o[1]} * i2), pk2(f32x2{o[2], o[3]} * i2), pk2(f32x2{o[4], o[5]} * i2), pk2(f32x2{o[6], o[7]} * i2)}, rsWb, oo + hd * 1024);
      }
    }
    DRAIN_BAR();
    if (w == 0) publish(rsWb, F3 + (rt * 8 + hg) * 8, tlo, thi, lane);
    STAMP(5);
  } else {
    v4u xr[2];
    v2u ep;
    {
      const u32 xo = hh * 1024 + l31 * 16;
      asm volatile("buffer_load_dwordx4 %0, %2, %3, %4 offen\n\tbuffer_load_dwordx4 %1, %2, %3, %4 offen offset:512"
                   : "=&v"(xr[0]), "=&v"(xr[1]) : "v"(xo), "s"(rsX), "s"((bid * 16 + 2 * w) * 1024) : "memory");
    }
    ld64_sc1_new(ep, 0u, rsW, WS_EPOCH);
    const u32 gown = (ft == 15 ? 43u : ((ft + 1) * 8) / 3) - g0;
    const u32 fd = w >= 6 ? 1u : 0u, gs = w - 6 * fd;
    v2u bias0, bias1;
    {
      const v4u rsB = mkrs(gb1, 1376 * 2);
      const u32 bo = (g0 * 32 + gs * 16 + 4 * hh) * 2;
      asm volatile("buffer_load_dwordx2 %0, %2, %3, 0 offen\n\tbuffer_load_dwordx2 %1, %2, %3, 0 offen offset:16" : "=&v"(bias0), "=&v"(bias1) : "v"(bo), "s"(rsB) : "memory");
    }
    asm volatile("s_waitcnt vmcnt(2)" : "+v"(xr[0]), "+v"(xr[1]), "+v"(ep)::"memory");
    STAMP(1);
    tlo = rfl(ep.x) + 1u; thi = rfl(ep.y);
    const u32 HIM = 147456;
    {
      f32x2 v[8];
#pragma unroll
      for (int j = 0; j < 4; ++j) { u32 a = xr[0][j], c = xr[1][j]; v[j] = unp(a); v[4 + j] = unp(c); }
      f32x2 s = ((v[0] + v[1]) + (v[2] + v[3])) + ((v[4] + v[5]) + (v[6] + v[7]));
      const float m = hsum32(s.x + s.y) * (1.f / 512.f);
      f32x2 q = {0.f, 0.f}, q1 = q;
#pragma unroll
      for (int j = 0; j < 8; j += 2) { v[j] -= spl(m); v[j + 1] -= spl(m); q = fma2(v[j], v[j], q); q1 = fma2(v[j + 1], v[j + 1], q1); }
      q += q1;
      const float r1 = __builtin_amdgcn_rsqf(fmaf(hsum32(q.x + q.y), 1.f / 512.f, 1e-5f));
      q = f32x2{0.f, 0.f}; q1 = q;
#pragma unroll
      for (int j = 0; j < 8; j += 2) { v[j] *= spl(r1); v[j + 1] *= spl(r1); q = fma2(v[j], v[j], q); q1 = fma2(v[j + 1], v[j + 1], q1); }
      q += q1;
      s = ((v[0] + v[1]) + (v[2] + v[3])) + ((v[4] + v[5]) + (v[6] + v[7]));
      const float m2 = hsum32(s.x + s.y) * (1.f / 512.f);
      const float v2 = fmaxf(fmaf(hsum32(q.x + q.y), 1.f / 512.f, -m2 * m2), 0.f);
      const float r2 = __builtin_amdgcn_rsqf(v2 + 1e-5f);
      const f32x2 rr = spl(r2), nb = spl(-m2 * r2);
      const u32 row = 2 * w + hh, ha = HIM + row * 1024 + ((l31 ^ ((row & 3) << 2)) << 4);
      sts128(ha, v4u{pk2(fma2(v[0], rr, nb)), pk2(fma2(v[1], rr, nb)), pk2(fma2(v[2], rr, nb)), pk2(fma2(v[3], rr, nb))});
      sts128(ha + 512, v4u{pk2(fma2(v[4], rr, nb)), pk2(fma2(v[5], rr, nb)), pk2(fma2(v[6], rr, nb)), pk2(fma2(v[7], rr, nb))});
      if (l31 == 0) st128_sc1(v4u{__float_as_uint(m), tlo, __float_as_uint(r1), tlo * 0x9E3779B1u + thi}, rsWb, WS_STATS + (bid * 16 + row) * 16);
    }
    STAMP(9);
    BAR();
#pragma unroll
    for (int i = 0; i < 2; ++i) {
      const u32 d32 = 2 * w + i;
      const u32 a = HIM + (8 * hh + q4) * 1024 + (((d32 * 4 + 2 * blk + (p4 >> 1)) ^ (q4 << 2)) << 4) + (p4 & 1) * 8;
      st128_sc1(trfrag(a, a + 4096), rsWb, WS_H + (d32 * 128 + bid) * 1024 + lane16);
    }
    STAMP(10);
    asm volatile("s_waitcnt vmcnt(0) lgkmcnt(0)\n\ts_barrier" : "+v"(bias0), "+v"(bias1)::"memory");
    if (w == 0) {
      publish(rsWb, F1 + bid * 8, tlo, thi, lane);
      STAMP(2);
      poll128(rsW, WS_FLAGS + xcd * FL_REP + F1 + lane16, tlo, thi);
    }
    LBAR();
    STAMP(3);
    v4u rsH = mkrs(ws + WS_H + ((d64 * 2 + dh) * 128 + kq * 32) * 1024, 32768);
    v4u hf[8];
#pragma unroll
    for (int i = 0; i < 8; ++i) ld128_sc1_new(hf[i], lane16, rsH, (brot + i) * 1024);
    f32x16 acc[3] = {zero16(), zero16(), zero16()};
    const u32 wa = kq * 6144 + (8 * hh + q4) * 192 + blk * 32 + p4 * 8;
    v4u wf[2][3];
    auto rd = [&](int buf, u32 a) {
#pragma unroll
      for (int g = 0; g < 3; ++g) wf[buf][g] = trfrag(a + g * 64, a + 768 + g * 64);
    };
    rd(0, wa);
    u32 sb = 0, so = brot * 1024;
#pragma unroll 1
    for (u32 r = 0; r < 4; ++r) {
      so = (so + 8192) & 32767;
      if (r == 3) rsH.z = 0;
#pragma unroll
      for (int i = 0; i < 8; ++i) {
        if (i & 1) { BAR(); sb = sb == 5 * 24576 ? 0u : sb + 24576; rd(0, sb + wa); }
        else rd(1, sb + wa + 3072);
        asm volatile("s_waitcnt vmcnt(7)" : "+v"(hf[i])::"memory");
        SCHED();
#pragma unroll
        for (int g = 0; g < 3; ++g) acc[g] = mfma(wf[i & 1][g], hf[i], acc[g]);
        ld128_sc1(hf[i], lane16, rsH, so + i * 1024);
        SCHED();
      }
    }
    STAMP(4);
    const u32 RED = 49152;
#pragma unroll
    for (int g = 0; g < 3; ++g)
#pragma unroll
      for (int qq = 0; qq < 4; ++qq)
        sts128f(RED + (w * 12 + g * 4 + qq) * 1024 + lane16, f32x4{acc[g][4 * qq], acc[g][4 * qq + 1], acc[g][4 * qq + 2], acc[g][4 * qq + 3]});
    asm volatile("s_waitcnt vmcnt(0) lgkmcnt(0)\n\ts_barrier" : "+v"(hf[0]), "+v"(hf[1]), "+v"(hf[2]), "+v"(hf[3]), "+v"(hf[4]), "+v"(hf[5]), "+v"(hf[6]), "+v"(hf[7])::"memory");
    {
      f32x4 s0 = {0.f, 0.f, 0.f, 0.f}, s1 = s0;
#pragma unroll
      for (int k = 0; k < 4; ++k) {
        u32 a = RED + ((k * 2 + fd) * 12 + gs * 2) * 1024 + lane16;
        s0 += lds128f(a); s1 += lds128f(a + 1024);
      }
      f32x2 v[4] = {f32x2{s0[0], s0[1]} + unp(bias0.x), f32x2{s0[2], s0[3]} + unp(bias0.y), f32x2{s1[0], s1[1]} + unp(bias1.x), f32x2{s1[2], s1[3]} + unp(bias1.y)};
      v4u o;
#pragma unroll
      for (int j = 0; j < 4; ++j) {
        f32x2 t = v[j] * fma2(v[j] * v[j], spl(-2.f * 0.7978845608028654f * 0.044715f * LOG2E), spl(-2.f * 0.7978845608028654f * LOG2E));
        f32x2 d = f32x2{__builtin_amdgcn_exp2f(t.x), __builtin_amdgcn_exp2f(t.y)} + spl(1.f);
        o[j] = pk2(v[j] * f32x2{__builtin_amdgcn_rcpf(d.x), __builtin_amdgcn_rcpf(d.y)});
      }
      if ((gs >> 1) < gown) st128_sc1(o, rsWb, WS_U + ((d64 * 2 + fd) * 86 + g0 * 2 + gs) * 1024 + lane16);
    }
    DRAIN_BAR();
    if (w == 0) publish(rsWb, F4 + (d64 * 16 + ft) * 8, tlo, thi, lane);
    STAMP(5);
  }

  const bool isWO = kq == 3;
  const u32 etok = tid >> 3, ec = tid & 7;
  v4u xres;
  u32 b2r;
  ld128_new(xres, ((tt * 64 + etok) * 512 + dt * 64 + ec * 8) * 2, rsX, 0u);
  {
    const v4u rsB2 = mkrs(gb2, 4096);
    asm volatile("buffer_load_ushort %0, %1, %2, 0 offen" : "=v"(b2r) : "v"((tt * 64 + etok) * 2), "s"(rsB2) : "memory");
  }
  if (w == 0) {
    poll64(rsW, WS_FLAGS + xcd * FL_REP + (lane < 16 ? F4 + dt * 128 + lane * 8 : F3 + (tt >> 1) * 64 + (lane & 7) * 8), tlo, thi);
    if (b == 0 && lane == 0) st64_sc1(v2u{tlo, thi}, rsWb, WS_EPOCH);
  }
  LBAR();
  STAMP(6);
  v4u rsA = isWO ? mkrs(ws + WS_O + (tt * 2 + dh) * 32768, 32768) : mkrs(ws + WS_U + ((dt * 2 + dh) * 86 + kq * 29) * 1024, kq == 2 ? 28 * 1024 : 29 * 1024);
  v4u af[8];
#pragma unroll
  for (int i = 0; i < 8; ++i) ld128_sc1_new(af[i], lane16, rsA, (frot + i) * 1024);
  f32x16 acc[2] = {zero16(), zero16()};
  const u32 fa = kq * 4096 + (4 * hh + q4) * 128 + ((q4 >> 1) << 6) + blk * 32 + p4 * 8;
  v4u wf[2][2];
  auto rd = [&](int buf, u32 a) {
    wf[buf][0] = trfrag(a, a + 1024);
    wf[buf][1] = trfrag(a ^ 64, (a ^ 64) + 1024);
  };
  rd(0, fa);
  u32 sb = 0, so = frot * 1024;
#pragma unroll 1
  for (u32 r = 0; r < 4; ++r) {
    so = (so + 8192) & 32767;
    if (r == 3) rsA.z = 0;
#pragma unroll
    for (int i = 0; i < 8; ++i) {
      if (i & 1) { BAR(); sb = sb == 8 * 16384 ? 0u : sb + 16384; rd(0, sb + fa); }
      else rd(1, sb + fa + 2048);
      asm volatile("s_waitcnt vmcnt(7)" : "+v"(af[i])::"memory");
      SCHED();
      acc[0] = mfma(af[i], wf[i & 1][0], acc[0]);
      acc[1] = mfma(af[i], wf[i & 1][1], acc[1]);
      ld128_sc1(af[i], lane16, rsA, so + i * 1024);
      SCHED();
    }
  }
  STAMP(7);
  if (isWO) {
#pragma unroll
    for (int i = 0; i < 2; ++i)
#pragma unroll
      for (int j = 0; j < 16; ++j) {
        const u32 tk = (j & 3) + 8 * (j >> 2);
        sts32f(49152 + (dh * 32 + 4 * hh + tk) * 256 + i * 128 + ((((l31 >> 2) ^ (4 * hh + (j & 3))) & 7) << 4) + (l31 & 3) * 4, acc[i][j]);
      }
  } else {
    const u32 pb = kq * 16384 + l31 * 256 + dh * 128;
#pragma unroll
    for (int i = 0; i < 2; ++i)
#pragma unroll
      for (int qq = 0; qq < 4; ++qq)
        sts128f(pb + i * 8192 + (((2 * qq + hh) ^ (l31 & 7)) << 4), f32x4{acc[i][4 * qq], acc[i][4 * qq + 1], acc[i][4 * qq + 2], acc[i][4 * qq + 3]});
  }
  asm volatile("s_waitcnt vmcnt(0) lgkmcnt(0)\n\ts_barrier"
               : "+v"(af[0]), "+v"(af[1]), "+v"(af[2]), "+v"(af[3]), "+v"(af[4]), "+v"(af[5]), "+v"(af[6]), "+v"(af[7]), "+v"(xres), "+v"(b2r)::"memory");
  {
    const u32 a0 = etok * 256 + (((2 * ec) ^ (etok & 7)) << 4), a1 = a0 ^ 16;
    const float b2v = blo(b2r);
    f32x4 y0 = lds128f(a0) + lds128f(a0 + 16384) + lds128f(a0 + 32768);
    f32x4 y1 = lds128f(a1) + lds128f(a1 + 16384) + lds128f(a1 + 32768);
    f32x4 s0 = lds128f(a0 + 49152), s1 = lds128f(a1 + 49152);
    f32x2 y[4] = {{y0[0], y0[1]}, {y0[2], y0[3]}, {y1[0], y1[1]}, {y1[2], y1[3]}};
    f32x2 s[4] = {{s0[0], s0[1]}, {s0[2], s0[3]}, {s1[0], s1[1]}, {s1[2], s1[3]}};
    v4u o;
#pragma unroll
    for (int j = 0; j < 4; ++j) {
      f32x2 yy = y[j] + spl(b2v), t = yy * spl(-LOG2E);
      f32x2 d = f32x2{__builtin_amdgcn_exp2f(t.x), __builtin_amdgcn_exp2f(t.y)} + spl(1.f);
      f32x2 g = yy * f32x2{__builtin_amdgcn_rcpf(d.x), __builtin_amdgcn_rcpf(d.y)};
      u32 xd = xres[j];
      o[j] = pk2(fma2(s[j], g, unp(xd)));
    }
    *(v4u*)(gout + (tt * 64 + etok) * 512 + dt * 64 + ec * 8) = o;
  }
  STAMP(8);
  u64 cyc1 = __builtin_amdgcn_s_memtime();
  if (tid == 0) {
    u64* sp = (u64*)(ws + WS_STAMP) + b * 16;
#pragma unroll
    for (int i = 0; i < 12; ++i) sp[i] = stamp[i];
    sp[12] = cyc1 - cyc0;
    u32* cp = (u32*)(ws + WS_STAMP + 524288) + b * 16;
#pragma unroll
    for (int i = 0; i < 12; ++i) cp[i] = cst[i];
  }
}

__global__ void k_spin(const unsigned long long* st){
  __shared__ unsigned long long v[256]; __shared__ unsigned long long z[256];
  int t=threadIdx.x; z[t]=st[t*16]; v[t]=st[t*16+1]; __syncthreads();
  if(t==0){ unsigned long long mn=~0ull; for(int i=0;i<256;++i) mn=z[i]<mn?z[i]:mn;
    unsigned long long sum=0,mx=0; int n=0; for(int i=0;i<256;++i){ bool a=(((i>>3)&1)==0); if((a)){ unsigned long long d=v[i]-mn; sum+=d; mx=d>mx?d:mx; ++n; } }
    unsigned long long T=(sum/n)*100; if(T>1000000) T=1000000;
    unsigned long long s=__builtin_amdgcn_s_memrealtime(); while(__builtin_amdgcn_s_memrealtime()-s<T) __builtin_amdgcn_s_sleep(2); }
}
extern "C" void kernel_launch(void* const* d_in, const int* in_sizes, int n_in, void* d_out, int out_size, void* d_ws, size_t ws_size,
                              hipStream_t stream) {
  (void)in_sizes; (void)n_in; (void)out_size; (void)ws_size;
  (void)hipFuncSetAttribute((const void*)fused_kernel, hipFuncAttributeMaxDynamicSharedMemorySize, LDS_BYTES);
  fused_kernel<<<256, 768, LDS_BYTES, stream>>>((const u16*)d_in[0], (const u16*)d_in[1], (const u16*)d_in[2], (const u16*)d_in[3],
                                                (const u16*)d_in[5], (const u16*)d_in[6], (const u16*)d_in[7], (const u16*)d_in[8],
                                                (u16*)d_out, (char*)d_ws);
  k_spin<<<1,256,0,stream>>>((const unsigned long long*)((char*)d_ws+WS_STAMP));
}
```

```cpp
#define STAMPS 1
#include <hip/hip_runtime.h>
#include <stdint.h>

typedef unsigned u32;
typedef unsigned short u16;
typedef unsigned long long u64;
typedef u32 v4u __attribute__((ext_vector_type(4)));
typedef u32 v2u __attribute__((ext_vector_type(2)));
typedef float f32x16 __attribute__((ext_vector_type(16)));
typedef float f32x4 __attribute__((ext_vector_type(4)));
typedef float f32x2 __attribute__((ext_vector_type(2)));
typedef short s16x4 __attribute__((ext_vector_type(4)));
typedef __bf16 bf16x8 __attribute__((ext_vector_type(8)));
typedef __bf16 bf16x2 __attribute__((ext_vector_type(2)));
#define LAS __attribute__((address_space(3)))
#define DI __device__ __forceinline__

#define HOLD 1

constexpr u32 WS_EPOCH = 0, WS_FLAGS = 4096, FL_REP = 4096;
constexpr u32 F1 = 0, F2 = 1024, F3 = 2048, F4 = 3072;
constexpr u32 WS_STATS = 65536;
constexpr u32 WS_KVZ = 131072;
constexpr u32 WS_KVR = 262144;
constexpr u32 WS_H = 1u << 20;
constexpr u32 WS_O = 3u << 20;
constexpr u32 WS_U = 5u << 20;
constexpr u32 WS_BYTES = 7u << 20;
constexpr u32 WS_STAMP = 8u << 20;
constexpr u32 LDS_BYTES = 163840;
constexpr u32 CODE_BYTES = 19 * 1024;
constexpr u32 TOUCH_W = 3;
constexpr u32 SPIN_CAP = 1u << 15;
constexpr float LOG2E = 1.4426950408889634f;

DI u32 rfl(u32 v) { return (u32)__builtin_amdgcn_readfirstlane((int)v); }
DI v4u mkrs(const void* p, u32 bytes) { u64 a = (u64)p; return v4u{(u32)a, (u32)(a >> 32) & 0xffffu, bytes, 0x00020000u}; }
DI __amdgpu_buffer_rsrc_t mkrsb(const void* p, u32 bytes) { return __builtin_amdgcn_make_buffer_rsrc((void*)p, (short)0, (int)bytes, 0x00020000); }
DI void gl4(u32 m, u32 v0, u32 v1, u32 v2, u32 v3, v4u rs, u32 sa, u32 sb) {
  asm volatile("s_mov_b32 m0, %0\n\ts_nop 0\n\t"
               "buffer_load_dwordx4 %1, %5, %6 offen lds\n\tbuffer_load_dwordx4 %2, %5, %6 offen offset:1024 lds\n\t"
               "buffer_load_dwordx4 %3, %5, %7 offen offset:2048 lds\n\tbuffer_load_dwordx4 %4, %5, %7 offen offset:3072 lds"
               ::"s"(m), "v"(v0), "v"(v1), "v"(v2), "v"(v3), "s"(rs), "s"(sa), "s"(sb) : "memory");
}
DI void gl3(u32 m, u32 v0, u32 v1, u32 v2, v4u rs, u32 sa) {
  asm volatile("s_mov_b32 m0, %0\n\ts_nop 0\n\t"
               "buffer_load_dwordx4 %1, %4, %5 offen lds\n\tbuffer_load_dwordx4 %2, %4, %5 offen offset:1024 lds\n\t"
               "buffer_load_dwordx4 %3, %4, %5 offen offset:2048 lds"
               ::"s"(m), "v"(v0), "v"(v1), "v"(v2), "s"(rs), "s"(sa) : "memory");
}
DI void ld128_sc1(v4u& d, u32 voff, v4u rs, u32 soff) {
  asm volatile("buffer_load_dwordx4 %0, %1, %2, %3 offen sc1" : "+v"(d) : "v"(voff), "s"(rs), "s"(soff) : "memory");
}
DI void ld128_sc1_new(v4u& d, u32 voff, v4u rs, u32 soff) {
  asm volatile("buffer_load_dwordx4 %0, %1, %2, %3 offen sc1" : "=v"(d) : "v"(voff), "s"(rs), "s"(soff) : "memory");
}
DI void ld128_new(v4u& d, u32 voff, v4u rs, u32 soff) {
  asm volatile("buffer_load_dwordx4 %0, %1, %2, %3 offen" : "=v"(d) : "v"(voff), "s"(rs), "s"(soff) : "memory");
}
DI void ld64_sc1_new(v2u& d, u32 voff, v4u rs, u32 soff) {
  asm volatile("buffer_load_dwordx2 %0, %1, %2, %3 offen sc1" : "=v"(d) : "v"(voff), "s"(rs), "s"(soff) : "memory");
}
DI void st128_sc1(v4u v, __amdgpu_buffer_rsrc_t rs, u32 off) { __builtin_amdgcn_raw_buffer_store_b128(v, rs, (int)off, 0, 16); }
DI void st64_sc1(v2u v, __amdgpu_buffer_rsrc_t rs, u32 off) { __builtin_amdgcn_raw_buffer_store_b64(v, rs, (int)off, 0, 16); }
#define BAR() asm volatile("s_waitcnt lgkmcnt(0)\n\ts_barrier" ::: "memory")
#define DRAIN_BAR() asm volatile("s_waitcnt vmcnt(0) lgkmcnt(0)\n\ts_barrier" ::: "memory")
#define VBAR(n) asm volatile("s_waitcnt vmcnt(" #n ")\n\ts_barrier" ::: "memory")
#define LBAR() asm volatile("s_barrier" ::: "memory")
#define SCHED() __builtin_amdgcn_sched_barrier(0)

DI v2u trrd(u32 a) { return __builtin_bit_cast(v2u, __builtin_amdgcn_ds_read_tr16_b64_v4i16((LAS s16x4*)(size_t)a)); }
DI v4u trfrag(u32 a0, u32 a1) { v2u x = trrd(a0), y = trrd(a1); return v4u{x.x, x.y, y.x, y.y}; }
DI v4u lds128(u32 a) { return *(LAS v4u*)(size_t)a; }
DI v2u lds64(u32 a) { return *(LAS v2u*)(size_t)a; }
DI f32x4 lds128f(u32 a) { return *(LAS f32x4*)(size_t)a; }
DI void sts128(u32 a, v4u v) { *(LAS v4u*)(size_t)a = v; }
DI void sts128f(u32 a, f32x4 v) { *(LAS f32x4*)(size_t)a = v; }
DI void sts64(u32 a, v2u v) { *(LAS v2u*)(size_t)a = v; }
DI void sts32f(u32 a, float v) { *(LAS float*)(size_t)a = v; }
DI f32x16 mfma(v4u a, v4u b, f32x16 c) {
  return __builtin_amdgcn_mfma_f32_32x32x16_bf16(__builtin_bit_cast(bf16x8, a), __builtin_bit_cast(bf16x8, b), c, 0, 0, 0);
}
DI f32x4 mfma16(v4u a, v4u b, f32x4 c) {
  return __builtin_amdgcn_mfma_f32_16x16x32_bf16(__builtin_bit_cast(bf16x8, a), __builtin_bit_cast(bf16x8, b), c, 0, 0, 0);
}
DI u32 pk2(f32x2 v) { return __builtin_bit_cast(u32, __builtin_convertvector(v, bf16x2)); }
DI u32 pk(float a, float b) { return pk2(f32x2{a, b}); }
DI float blo(u32 v) { return __uint_as_float(v << 16); }
DI float bhi(u32 v) { return __uint_as_float(v & 0xffff0000u); }
DI f32x2 unp(u32 v) { return f32x2{blo(v), bhi(v)}; }
DI f32x2 fma2(f32x2 a, f32x2 b, f32x2 c) { return __builtin_elementwise_fma(a, b, c); }
DI f32x2 spl(float a) { return f32x2{a, a}; }
DI float dot2(u32 a, u32 b, float c) { return __builtin_amdgcn_fdot2_f32_bf16(__builtin_bit_cast(bf16x2, a), __builtin_bit_cast(bf16x2, b), c, false); }
template <int C> DI float dpp(float v) { return __int_as_float(__builtin_amdgcn_mov_dpp(__float_as_int(v), C, 0xF, 0xF, true)); }
DI float x16sum(float v) { u32 x = __float_as_uint(v); auto r = __builtin_amdgcn_permlane16_swap(x, x, false, false); return __uint_as_float(r[0]) + __uint_as_float(r[1]); }
DI float x32sum(float v) { u32 x = __float_as_uint(v); auto r = __builtin_amdgcn_permlane32_swap(x, x, false, false); return __uint_as_float(r[0]) + __uint_as_float(r[1]); }
DI float x32max(float v) { u32 x = __float_as_uint(v); auto r = __builtin_amdgcn_permlane32_swap(x, x, false, false); return fmaxf(__uint_as_float(r[0]), __uint_as_float(r[1])); }
DI float x32min(float v) { u32 x = __float_as_uint(v); auto r = __builtin_amdgcn_permlane32_swap(x, x, false, false); return fminf(__uint_as_float(r[0]), __uint_as_float(r[1])); }
DI float hsum32(float v) {
  v += dpp<0xB1>(v); v += dpp<0x4E>(v); v += dpp<0x141>(v); v += dpp<0x140>(v);
  return x16sum(v);
}
DI f32x16 zero16() { f32x16 z;
#pragma unroll
  for (int j = 0; j < 16; ++j) z[j] = 0.f;
  return z; }

DI void publish(__amdgpu_buffer_rsrc_t rsW, u32 off, u32 tlo, u32 thi, u32 lane) {
  if (lane < 8) st64_sc1(v2u{tlo, thi}, rsW, WS_FLAGS + lane * FL_REP + off);
}
DI void poll64(v4u rsW, u32 off, u32 tlo, u32 thi) {
#pragma unroll 1
  for (u32 spin = 0; spin < SPIN_CAP; ++spin) {
    v2u t;
    asm volatile("buffer_load_dwordx2 %0, %1, %2, 0 offen sc1\n\ts_waitcnt vmcnt(0)" : "=&v"(t) : "v"(off), "s"(rsW) : "memory");
    if (__all(t.x == tlo && t.y == thi)) break;
  }
}
DI void poll128(v4u rsW, u32 off, u32 tlo, u32 thi) {
#pragma unroll 1
  for (u32 spin = 0; spin < SPIN_CAP; ++spin) {
    v4u t;
    asm volatile("buffer_load_dwordx4 %0, %1, %2, 0 offen sc1\n\ts_waitcnt vmcnt(0)" : "=&v"(t) : "v"(off), "s"(rsW) : "memory");
    if (__all(t.x == tlo && t.y == thi && t.z == tlo && t.w == thi)) break;
  }
}

DI void dual_softmax(const f32x16& z, int o, float c, v4u& fp, v4u& fn) {
  f32x2 p[4] = {{z[o], z[o + 1]}, {z[o + 2], z[o + 3]}, {z[o + 4], z[o + 5]}, {z[o + 6], z[o + 7]}};
  float mx = fmaxf(fmaxf(fmaxf(p[0].x, p[0].y), p[1].x), fmaxf(fmaxf(p[1].y, p[2].x), p[2].y));
  mx = fmaxf(fmaxf(mx, p[3].x), p[3].y);
  float mn = fminf(fminf(fminf(p[0].x, p[0].y), p[1].x), fminf(fminf(p[1].y, p[2].x), p[2].y));
  mn = fminf(fminf(mn, p[3].x), p[3].y);
  mx = x32max(mx); mn = x32min(mn);
  const f32x2 a = spl(-mx * c), b = spl(mn * c), c2 = spl(c), n2 = spl(-c);
  f32x2 e[4], g[4];
#pragma unroll
  for (int i = 0; i < 4; ++i) {
    f32x2 t = fma2(p[i], c2, a), u = fma2(p[i], n2, b);
    e[i] = f32x2{__builtin_amdgcn_exp2f(t.x), __builtin_amdgcn_exp2f(t.y)};
    g[i] = f32x2{__builtin_amdgcn_exp2f(u.x), __builtin_amdgcn_exp2f(u.y)};
  }
  f32x2 se = (e[0] + e[1]) + (e[2] + e[3]), sg = (g[0] + g[1]) + (g[2] + g[3]);
  const f32x2 ie = spl(__builtin_amdgcn_rcpf(x32sum(se.x + se.y))), ig = spl(__builtin_amdgcn_rcpf(x32sum(sg.x + sg.y)));
  fp = v4u{pk2(e[0] * ie), pk2(e[1] * ie), pk2(e[2] * ie), pk2(e[3] * ie)};
  fn = v4u{pk2(g[0] * ig), pk2(g[1] * ig), pk2(g[2] * ig), pk2(g[3] * ig)};
}

#define STAMP(i) do { stamp[i] = __builtin_amdgcn_s_memrealtime(); cst[i] = (u32)__builtin_amdgcn_s_memtime(); } while (0)

#define TS(base, n) do {} while (0)
#define TSDUMP(base, sec) do {} while (0)
#define VBART(n, base, k) VBAR(n)
__global__ __launch_bounds__(768, 3) void fused_kernel(const u16* __restrict__ gx, const u16* __restrict__ gwqkv, const u16* __restrict__ gwphi,
                                                         const u16* __restrict__ gwo, const u16* __restrict__ gw1, const u16* __restrict__ gb1,
                                                         const u16* __restrict__ gw2, const u16* __restrict__ gb2, u16* __restrict__ gout, char* ws) {
  u64 stamp[12]; u32 cst[12];
#pragma unroll
  for (int i = 0; i < 12; ++i) { stamp[i] = 0; cst[i] = 0; }
  u64 cyc0 = __builtin_amdgcn_s_memtime();
  u64 pc0;
  asm volatile("s_getpc_b64 %0" : "=s"(pc0));
  STAMP(0);
  const u32 tid = threadIdx.x, lane = tid & 63;
  const u32 w = rfl(tid >> 6);
  const u32 b = blockIdx.x, xcd = b & 7, idx = b >> 3, li = idx >> 1;
  const bool roleA = (idx & 1) == 0;
  const u32 lane16 = lane * 16;
  const u32 tt = xcd * 4 + (idx >> 3), dt = idx & 7;
  const u32 frot = (((idx >> 3) + (dt >> 1)) * 8) & 31;
  const u32 rt = (xcd >> 1) * 4 + (li >> 2), hg = (xcd & 1) * 4 + (li & 3);
  const u32 bid = xcd * 16 + li;
  const u32 d64 = (xcd & 1) * 4 + (li & 3), ft = (xcd >> 1) * 4 + (li >> 2);
  const u32 g0 = (ft * 8) / 3;
  const u32 brot = (((li & 3) + (li >> 2)) * 8) & 31;

  if (w >= 8) {
    const u32 lw = w - 8;
    const v4u rsC = mkrs((const void*)(pc0 & ~(u64)255), CODE_BYTES);
    const u32 cvo = (li * 16 + lw * 4 + (lane & 3)) * 128 + (lane >> 2) * (64 * 1024);
    auto touch = [&](u32 la) {
      asm volatile("s_mov_b32 m0, %0\n\ts_nop 0\n\tbuffer_load_dword %1, %2, 0 offen lds" ::"s"(la), "v"(cvo), "s"(rsC) : "memory");
    };
    touch(roleA ? 150528u : 5 * 24576 + lw * 6144);
    const bool wo = lw == 3;
    const u32 fpitch = wo ? 1024u : 4096u, fsh = wo ? 14u : 16u;
    const u32 fcol = wo ? dt * 128 : tt * 128, fcnt = wo ? 32u : (lw == 2 ? 28u : 29u);
    const char* fbase = wo ? (const char*)gwo : (const char*)gw2 + (size_t)(lw * 29) * 65536;
    const v4u rsF = mkrs(fbase + fcol - 4096, (fcnt << fsh) - fcol + 4096);
    const u32 fv0 = (lane >> 3) * fpitch + (((lane & 7) ^ (((lane >> 4) & 1) << 2)) << 4) + 4096;
    const u32 fv1 = fv0 + 8 * fpitch - 1024, fv2 = fv0 - 2048, fv3 = fv1 - 2048;
    auto fin = [&](u32 st, u32 slot) {
      u32 i = (2 * st + frot) & 31;
      gl4(slot * 16384 + lw * 4096, fv0, fv1, fv2, fv3, rsF, i << fsh, (i + 1) << fsh);
    };
    if (roleA) {
      const v4u rsX = mkrs((const char*)gx - 4096, 2048 * 1024 + 4096), rsQ = mkrs((const char*)gwqkv - 4096, 512 * 3072 + 4096);
      const u32 rot = (2 * ((li >> 2) + (li & 3))) & 7;
      u32 xv[4], wv[6];
#pragma unroll
      for (int i = 0; i < 4; ++i) {
        u32 row = 32 * lw + 8 * i + (lane >> 3);
        xv[i] = (rt * 128 + row) * 1024 + (((lane & 7) ^ ((row >> 1) & 7)) << 4) + 4096 - i * 1024;
      }
#pragma unroll
      for (int i = 0; i < 3; ++i) {
        u32 o = i * 1024 + lane16, row = o / 384, c = (o - row * 384) >> 4;
        wv[i] = (16 * lw + row) * 3072 + (c >> 3) * 1024 + hg * 128 + (((c & 7) ^ (((row >> 1) & 1) << 2)) << 4) + 4096 - i * 1024;
        wv[3 + i] = wv[i] + 8 * 3072;
      }
      auto stage = [&](u32 st, u32 slot) {
        u32 k = (st + rot) & 7, la = slot * 40960;
        gl4(la + lw * 4096, xv[0], xv[1], xv[2], xv[3], rsX, k * 128, k * 128);
        gl3(la + 16384 + lw * 6144, wv[0], wv[1], wv[2], rsQ, k * (64 * 3072));
        gl3(la + 16384 + lw * 6144 + 3072, wv[3], wv[4], wv[5], rsQ, k * (64 * 3072));
      };
      stage(0, 0); stage(1, 1); stage(2, 2);
      VBAR(20);
      u32 slot = 0;
#pragma unroll 1
      for (u32 j = 0; j < 5; ++j) { VBART(10, 131072, j); stage(j + 3, slot); TS(131072, 3 * j + 2); slot = slot == 2 ? 0u : slot + 1; }
      touch(150528u);
      VBART(11, 131072, 5); VBART(1, 131072, 6); LBAR(); TS(131072, 22); TSDUMP(131072, 0);
      fin(0, 0); fin(1, 1);
      LBAR(); LBAR(); LBAR();
      touch(150528u);
      fin(2, 2); fin(3, 3);
      LBAR(); LBAR();
      fin(4, 4); fin(5, 5); fin(6, 6); fin(7, 7); fin(8, 8);
      LBAR();
    } else {
      const v4u rs1 = mkrs((const char*)gw1 + (size_t)(lw * 512) * 2752 + g0 * 64 - 4096, 512 * 2752 - g0 * 64 + 4096);
      u32 wv[3];
#pragma unroll
      for (int i = 0; i < 3; ++i) { u32 o = i * 1024 + lane16, row = o / 192; wv[i] = row * 2752 + (o - row * 192) + 4096 - i * 1024; }
      auto stage = [&](u32 st, u32 slot) {
        u32 so = ((2 * st + brot) & 31) * (16 * 2752), la = slot * 24576 + lw * 6144;
        gl3(la, wv[0], wv[1], wv[2], rs1, so);
        gl3(la + 3072, wv[0], wv[1], wv[2], rs1, so + 16 * 2752);
      };
      LBAR();
      LBAR();
      stage(0, 0); stage(1, 1); stage(2, 2); stage(3, 3); stage(4, 4); stage(5, 5);
      VBAR(30);
      u32 slot = 0;
#pragma unroll 1
      for (u32 j = 0; j < 10; ++j) { VBART(24, 155648, j); stage(j + 6, slot); TS(155648, 3 * j + 2); slot = slot == 5 ? 0u : slot + 1; }
      touch(150528u);
      VBART(25, 155648, 10); VBART(19, 155648, 11); VBART(13, 155648, 12); VBART(7, 155648, 13);
      fin(0, 0); fin(1, 1); fin(2, 2);
      VBART(13, 155648, 14); LBAR(); TS(155648, 46); TSDUMP(155648, 0);
      LBAR();
      {
        const __amdgpu_buffer_rsrc_t rsWb = mkrsb(ws, WS_BYTES);
        const u32 gown = (ft == 15 ? 43u : ((ft + 1) * 8) / 3) - g0;
        const u32 gs = 2 + lw, hh = lane >> 5;
        const u16* p = gb1 + g0 * 32 + gs * 16 + 4 * hh;
        const v2u b0 = *(const v2u*)p, b1 = *(const v2u*)(p + 8);
        f32x4 s0 = {0.f, 0.f, 0.f, 0.f}, s1 = s0;
#pragma unroll
        for (int k = 0; k < 4; ++k) {
          u32 a = 49152 + ((k * 2 + 1) * 12 + gs * 2) * 1024 + lane16;
          s0 += lds128f(a); s1 += lds128f(a + 1024);
        }
        f32x2 v[4] = {f32x2{s0[0], s0[1]} + unp(b0.x), f32x2{s0[2], s0[3]} + unp(b0.y), f32x2{s1[0], s1[1]} + unp(b1.x), f32x2{s1[2], s1[3]} + unp(b1.y)};
        v4u o;
#pragma unroll
        for (int j = 0; j < 4; ++j) {
          f32x2 t = v[j] * fma2(v[j] * v[j], spl(-2.f * 0.7978845608028654f * 0.044715f * LOG2E), spl(-2.f * 0.7978845608028654f * LOG2E));
          f32x2 d = f32x2{__builtin_amdgcn_exp2f(t.x), __builtin_amdgcn_exp2f(t.y)} + spl(1.f);
          o[j] = pk2(v[j] * f32x2{__builtin_amdgcn_rcpf(d.x), __builtin_amdgcn_rcpf(d.y)});
        }
        if ((gs >> 1) < gown) st128_sc1(o, rsWb, WS_U + ((d64 * 2 + 1) * 86 + g0 * 2 + gs) * 1024 + lane16);
      }
      DRAIN_BAR();
      fin(3, 3); fin(4, 4); fin(5, 5); fin(6, 6); fin(7, 7); fin(8, 8);
    }
    VBAR(32);
#pragma unroll 1
    for (u32 j = 0; j < 7; ++j) { VBART(28, 155648, j); fin(j + 9, j); TS(155648, 3 * j + 2); }
    touch(150528u);
    VBART(29, 155648, 7); VBART(25, 155648, 8); VBART(21, 155648, 9); VBART(17, 155648, 10); VBART(13, 155648, 11); VBART(9, 155648, 12); VBART(5, 155648, 13); VBART(1, 155648, 14);
    LBAR(); TS(155648, 46); TSDUMP(155648, 1); LBAR();
    return;
  }

  const u32 l31 = lane & 31, hh = lane >> 5, blk = (lane >> 4) & 1, q4 = (lane & 15) >> 2, p4 = lane & 3;
  const v4u rsW = mkrs(ws, WS_BYTES);
  const __amdgpu_buffer_rsrc_t rsWb = mkrsb(ws, WS_BYTES);
  const v4u rsX = mkrs(gx, 2048 * 1024);
  const u32 dh = w & 1, kq = w >> 1;
  u32 tlo, thi;

  if (roleA) {
    v2u ep;
    ld64_sc1_new(ep, 0u, rsW, WS_EPOCH);
    const u32 tg = w & 3, hp = w >> 2;
    v4u wph;
    {
      const u16* p = gwphi + (hg * 4 + hp * 2 + (l31 >> 4)) * 256 + (4 * hh) * 16 + (l31 & 15);
      wph.x = p[0] | ((u32)p[16] << 16); wph.y = p[32] | ((u32)p[48] << 16);
      wph.z = p[128] | ((u32)p[144] << 16); wph.w = p[160] | ((u32)p[176] << 16);
    }
    const u32 xa0 = (32 * tg + l31) * 128 + ((hh ^ ((l31 >> 1) & 7)) << 4);
    const u32 wa = 16384 + (8 * hh + q4) * 384 + ((hp ^ (q4 >> 1)) << 6) + blk * 32 + p4 * 8;
    f32x16 acc[3] = {zero16(), zero16(), zero16()};
    float cs[3] = {0.f, 0.f, 0.f};
    v4u stg = {0u, 0u, 0u, 0u};
    const u32 stoff = WS_STATS + (rt * 128 + 32 * tg + l31) * 16;
    v4u xf[2], wf[2][3];
    auto rd = [&](int buf, u32 sb, int ks) {
      xf[buf] = lds128(sb + (xa0 ^ (ks << 5)));
#pragma unroll
      for (int s = 0; s < 3; ++s) wf[buf][s] = trfrag(sb + wa + ks * 6144 + s * 128, sb + wa + ks * 6144 + 1536 + s * 128);
    };
    BAR();
    STAMP(1);
    rd(0, 0, 0);
    u32 sb = 0;
#pragma unroll 1
    for (u32 st = 0; st < 8; ++st) {
#pragma unroll
      for (int ks = 0; ks < 4; ++ks) {
        const int c = ks & 1;
        if (ks == 3) { BAR(); sb = sb == 81920 ? 0u : sb + 40960; rd(c ^ 1, sb, 0); }
        else rd(c ^ 1, sb, ks + 1);
        SCHED();
#pragma unroll
        for (int s = 0; s < 3; ++s) acc[s] = mfma(wf[c][s], xf[c], acc[s]);
        if (tg == (u32)ks) {
#pragma unroll
          for (int s = 0; s < 3; ++s) {
            u32 a0 = wf[c][s].x, a1 = wf[c][s].y, a2 = wf[c][s].z, a3 = wf[c][s].w;
            cs[s] = dot2(a0, 0x3f803f80u, cs[s]); cs[s] = dot2(a1, 0x3f803f80u, cs[s]);
            cs[s] = dot2(a2, 0x3f803f80u, cs[s]); cs[s] = dot2(a3, 0x3f803f80u, cs[s]);
          }
        }
        if (ks == 0 && st == 7) ld128_sc1(stg, stoff, rsW, 0u);
        SCHED();
      }
    }
    STAMP(2);
    const u32 CSP = 147456;
#pragma unroll
    for (int s = 0; s < 3; ++s) sts32f(CSP + (hp * 3 + s) * 512 + l31 * 16 + tg * 4, x32sum(cs[s]));
    asm volatile("s_waitcnt vmcnt(0) lgkmcnt(0)\n\ts_barrier" : "+v"(stg), "+v"(ep)::"memory");
    tlo = rfl(ep.x) + 1u; thi = rfl(ep.y);
    const u32 tB = tlo * 0x9E3779B1u + thi;
    v4u ca[3];
#pragma unroll
    for (int s = 0; s < 3; ++s) {
      f32x4 c4 = lds128f(CSP + (hp * 3 + s) * 512 + l31 * 16);
      float c = (c4[0] + c4[1]) + (c4[2] + c4[3]);
      u32 hi = pk(c, c);
      ca[s] = v4u{pk(c, c - blo(hi)), hi, 0u, 0u};
    }
#pragma unroll 1
    for (u32 spin = 0; spin < SPIN_CAP; ++spin) {
      if (__all(stg.y == tlo && stg.w == tB)) break;
      asm volatile("buffer_load_dwordx4 %0, %1, %2, 0 offen sc1\n\ts_waitcnt vmcnt(0)" : "+v"(stg) : "v"(stoff), "s"(rsW) : "memory");
    }
    const float rstd = __uint_as_float(stg.z), cexp = rstd * LOG2E;
    {
      const float nm = hh ? 0.f : -__uint_as_float(stg.x);
      const u32 mh = pk(nm, nm);
      const v4u mb = {mh, pk(nm - blo(mh), 0.f), 0u, 0u};
      acc[1] = mfma(ca[1], mb, acc[1]); acc[2] = mfma(ca[2], mb, acc[2]); acc[0] = mfma(ca[0], mb, acc[0]);
    }
    const bool lo16 = l31 < 16;
    const v4u wph0 = {lo16 ? wph.x : 0u, lo16 ? wph.y : 0u, lo16 ? wph.z : 0u, lo16 ? wph.w : 0u};
    const v4u wph1 = {wph.x ^ wph0.x, wph.y ^ wph0.y, wph.z ^ wph0.z, wph.w ^ wph0.w};
    auto logits = [&](const f32x16& a) {
      v4u f0 = {pk(a[0], a[1]), pk(a[2], a[3]), pk(a[4], a[5]), pk(a[6], a[7])};
      v4u f1 = {pk(a[8], a[9]), pk(a[10], a[11]), pk(a[12], a[13]), pk(a[14], a[15])};
      return mfma(wph1, f1, mfma(wph0, f0, zero16()));
    };
    const u32 IMG = 98304;
    {
      f32x16 pkk = logits(acc[1]);
      const u32 sw = ((l31 >> 1) & 1) | ((((l31 >> 2) ^ (l31 >> 3)) & 1) << 1);
      const u32 wa2 = IMG + w * 6144 + l31 * 64 + hh * 8 + (sw << 4);
      const f32x2 r2 = spl(rstd);
#pragma unroll
      for (int qq = 0; qq < 4; ++qq)
        sts64((wa2 ^ (qq << 4)) + 4096, v2u{pk2(f32x2{acc[2][4 * qq], acc[2][4 * qq + 1]} * r2), pk2(f32x2{acc[2][4 * qq + 2], acc[2][4 * qq + 3]} * r2)});
#pragma unroll
      for (int hd = 0; hd < 2; ++hd) {
        v4u fp, fn;
        dual_softmax(pkk, 8 * hd, cexp, fp, fn);
        sts64((wa2 ^ 0) + hd * 2048, v2u{fp.x, fp.y});
        sts64((wa2 ^ 16) + hd * 2048, v2u{fp.z, fp.w});
        sts64((wa2 ^ 32) + hd * 2048, v2u{fn.x, fn.y});
        sts64((wa2 ^ 48) + hd * 2048, v2u{fn.z, fn.w});
      }
    }
    STAMP(9);
    BAR();
    const v4u ones = {0x3f803f80u, 0x3f803f80u, 0x3f803f80u, 0x3f803f80u};
    {
      const u32 g = lane >> 4, hd = (w >> 1) & 1;
      u32 ra[2];
#pragma unroll
      for (int t = 0; t < 2; ++t) {
        u32 row = 8 * g + 4 * t + q4;
        u32 sw = ((row >> 1) & 1) | ((((row >> 2) ^ (row >> 3)) & 1) << 1);
        ra[t] = IMG + (w >> 2) * 24576 + row * 64 + (((p4 >> 1) ^ sw) << 4) + (p4 & 1) * 8;
      }
      const u32 oa = hd * 2048, xa = (w & 1) << 5, xb = hd << 5;
      f32x4 kv = {0.f, 0.f, 0.f, 0.f}, zz = kv;
#pragma unroll
      for (int t4 = 0; t4 < 4; ++t4) {
        v4u af = trfrag((ra[0] ^ xa) + oa + t4 * 6144, (ra[1] ^ xa) + oa + t4 * 6144);
        v4u vf = trfrag((ra[0] ^ xb) + 4096 + t4 * 6144, (ra[1] ^ xb) + 4096 + t4 * 6144);
        kv = mfma16(af, vf, kv); zz = mfma16(af, ones, zz);
      }
      st64_sc1(v2u{pk(kv[0], kv[1]), pk(kv[2], kv[3])}, rsWb, WS_KVR + ((hg * 8 + w) * 16 + rt) * 512 + lane * 8);
      if ((lane & 15) == 0) st64_sc1(v2u{pk(zz[0], zz[1]), pk(zz[2], zz[3])}, rsWb, WS_KVZ + ((hg * 8 + w) * 16 + rt) * 32 + g * 8);
    }
    STAMP(10);
    DRAIN_BAR();
    if (w == 0) publish(rsWb, F2 + (hg * 16 + rt) * 8, tlo, thi, lane);
    STAMP(3);
    v4u pq[2][2];
    {
      f32x16 pqq = logits(acc[0]);
      dual_softmax(pqq, 0, cexp, pq[0][0], pq[0][1]);
      dual_softmax(pqq, 8, cexp, pq[1][0], pq[1][1]);
    }
    if (w == 0) poll64(rsW, WS_FLAGS + xcd * FL_REP + F2 + hg * 128 + (lane & 15) * 8, tlo, thi);
    LBAR();
    STAMP(4);
    const u32 KZ = 151552, ZR = 159744;
    {
      v4u pr[8], pz;
      ld128_sc1_new(pz, l31 * 16, rsW, WS_KVZ + (hg * 8 + w) * 512);
#pragma unroll
      for (int i = 0; i < 8; ++i) ld128_sc1_new(pr[i], lane16, rsW, WS_KVR + (hg * 8 + w) * 8192 + i * 1024);
      asm volatile("s_waitcnt vmcnt(8)" : "+v"(pz)::"memory");
      sts128(ZR + w * 512 + l31 * 16, pz);
      const u32 g = lane >> 4, za = ZR + w * 512 + (8 * (g & 1) + q4) * 32 + p4 * 8;
      const u32 on = hh ? 0u : 0x3f803f80u;
      f32x4 zs = mfma16(trfrag(za, za + 128), v4u{on, on, on, on}, f32x4{0.f, 0.f, 0.f, 0.f});
      sts64(KZ + w * 1024 + 512 + (g >> 1) * 256 + (g & 1) * 8, v2u{pk(zs[0], zs[1]), pk(zs[2], zs[3])});
      float sm[8];
      u32 klo = 0x00003f80u;
      asm("" : "+s"(klo));
#pragma unroll
      for (int j = 0; j < 8; ++j) sm[j] = 0.f;
      asm volatile("s_waitcnt vmcnt(4)" : "+v"(pr[0]), "+v"(pr[1]), "+v"(pr[2]), "+v"(pr[3])::"memory");
#pragma unroll
      for (int i = 0; i < 8; ++i) {
        if (i == 4) asm volatile("s_waitcnt vmcnt(0)" : "+v"(pr[4]), "+v"(pr[5]), "+v"(pr[6]), "+v"(pr[7])::"memory");
        u32 d0 = pr[i].x, d1 = pr[i].y, d2 = pr[i].z, d3 = pr[i].w;
        sm[0] = dot2(d0, klo, sm[0]); sm[1] = dot2(d0, 0x3f800000u, sm[1]);
        sm[2] = dot2(d1, klo, sm[2]); sm[3] = dot2(d1, 0x3f800000u, sm[3]);
        sm[4] = dot2(d2, klo, sm[4]); sm[5] = dot2(d2, 0x3f800000u, sm[5]);
        sm[6] = dot2(d3, klo, sm[6]); sm[7] = dot2(d3, 0x3f800000u, sm[7]);
      }
      float t[4];
#pragma unroll
      for (int j = 0; j < 4; ++j) {
        auto r = __builtin_amdgcn_permlane32_swap(__float_as_uint(sm[j]), __float_as_uint(sm[j + 4]), false, false);
        t[j] = __uint_as_float(r[0]) + __uint_as_float(r[1]);
      }
      sts64(KZ + w * 1024 + (2 * l31 + hh) * 8, v2u{pk(t[0], t[1]), pk(t[2], t[3])});
    }
    BAR();
    {
      const u32 ka = KZ + hp * 4096 + (lo16 ? (hh * 16 + l31) * 8 : 512 + hh * 8);
      const u32 oo = WS_O + ((rt * 4 + tg) * 32 + hg * 4 + hp * 2) * 1024 + lane16;
#pragma unroll
      for (int hd = 0; hd < 2; ++hd) {
        f32x16 o = zero16();
#pragma unroll
        for (int s = 0; s < 2; ++s) {
          v2u x = lds64(ka + (hd * 2 + s) * 1024), y = lds64(ka + (hd * 2 + s) * 1024 + 256);
          o = mfma(v4u{x.x, x.y, y.x, y.y}, pq[hd][s], o);
        }
        const f32x2 i2 = spl(__builtin_amdgcn_rcpf(o[8] + 1e-6f));
        st128_sc1(v4u{pk2(f32x2{o[0], o[1]} * i2), pk2(f32x2{o[2], o[3]} * i2), pk2(f32x2{o[4], o[5]} * i2), pk2(f32x2{o[6], o[7]} * i2)}, rsWb, oo + hd * 1024);
      }
    }
    DRAIN_BAR();
    if (w == 0) publish(rsWb, F3 + (rt * 8 + hg) * 8, tlo, thi, lane);
    STAMP(5);
  } else {
    v4u xr[2];
    v2u ep;
    {
      const u32 xo = hh * 1024 + l31 * 16;
      asm volatile("buffer_load_dwordx4 %0, %2, %3, %4 offen\n\tbuffer_load_dwordx4 %1, %2, %3, %4 offen offset:512"
                   : "=&v"(xr[0]), "=&v"(xr[1]) : "v"(xo), "s"(rsX), "s"((bid * 16 + 2 * w) * 1024) : "memory");
    }
    ld64_sc1_new(ep, 0u, rsW, WS_EPOCH);
    const u32 gown = (ft == 15 ? 43u : ((ft + 1) * 8) / 3) - g0;
    const u32 fd = w >= 6 ? 1u : 0u, gs = w - 6 * fd;
    v2u bias0, bias1;
    {
      const v4u rsB = mkrs(gb1, 1376 * 2);
      const u32 bo = (g0 * 32 + gs * 16 + 4 * hh) * 2;
      asm volatile("buffer_load_dwordx2 %0, %2, %3, 0 offen\n\tbuffer_load_dwordx2 %1, %2, %3, 0 offen offset:16" : "=&v"(bias0), "=&v"(bias1) : "v"(bo), "s"(rsB) : "memory");
    }
    asm volatile("s_waitcnt vmcnt(2)" : "+v"(xr[0]), "+v"(xr[1]), "+v"(ep)::"memory");
    STAMP(1);
    tlo = rfl(ep.x) + 1u; thi = rfl(ep.y);
    const u32 HIM = 147456;
    {
      f32x2 v[8];
#pragma unroll
      for (int j = 0; j < 4; ++j) { u32 a = xr[0][j], c = xr[1][j]; v[j] = unp(a); v[4 + j] = unp(c); }
      f32x2 s = ((v[0] + v[1]) + (v[2] + v[3])) + ((v[4] + v[5]) + (v[6] + v[7]));
      const float m = hsum32(s.x + s.y) * (1.f / 512.f);
      f32x2 q = {0.f, 0.f}, q1 = q;
#pragma unroll
      for (int j = 0; j < 8; j += 2) { v[j] -= spl(m); v[j + 1] -= spl(m); q = fma2(v[j], v[j], q); q1 = fma2(v[j + 1], v[j + 1], q1); }
      q += q1;
      const float r1 = __builtin_amdgcn_rsqf(fmaf(hsum32(q.x + q.y), 1.f / 512.f, 1e-5f));
      q = f32x2{0.f, 0.f}; q1 = q;
#pragma unroll
      for (int j = 0; j < 8; j += 2) { v[j] *= spl(r1); v[j + 1] *= spl(r1); q = fma2(v[j], v[j], q); q1 = fma2(v[j + 1], v[j + 1], q1); }
      q += q1;
      s = ((v[0] + v[1]) + (v[2] + v[3])) + ((v[4] + v[5]) + (v[6] + v[7]));
      const float m2 = hsum32(s.x + s.y) * (1.f / 512.f);
      const float v2 = fmaxf(fmaf(hsum32(q.x + q.y), 1.f / 512.f, -m2 * m2), 0.f);
      const float r2 = __builtin_amdgcn_rsqf(v2 + 1e-5f);
      const f32x2 rr = spl(r2), nb = spl(-m2 * r2);
      const u32 row = 2 * w + hh, ha = HIM + row * 1024 + ((l31 ^ ((row & 3) << 2)) << 4);
      sts128(ha, v4u{pk2(fma2(v[0], rr, nb)), pk2(fma2(v[1], rr, nb)), pk2(fma2(v[2], rr, nb)), pk2(fma2(v[3], rr, nb))});
      sts128(ha + 512, v4u{pk2(fma2(v[4], rr, nb)), pk2(fma2(v[5], rr, nb)), pk2(fma2(v[6], rr, nb)), pk2(fma2(v[7], rr, nb))});
      if (l31 == 0) st128_sc1(v4u{__float_as_uint(m), tlo, __float_as_uint(r1), tlo * 0x9E3779B1u + thi}, rsWb, WS_STATS + (bid * 16 + row) * 16);
    }
    STAMP(9);
    BAR();
#pragma unroll
    for (int i = 0; i < 2; ++i) {
      const u32 d32 = 2 * w + i;
      const u32 a = HIM + (8 * hh + q4) * 1024 + (((d32 * 4 + 2 * blk + (p4 >> 1)) ^ (q4 << 2)) << 4) + (p4 & 1) * 8;
      st128_sc1(trfrag(a, a + 4096), rsWb, WS_H + (d32 * 128 + bid) * 1024 + lane16);
    }
    STAMP(10);
    asm volatile("s_waitcnt vmcnt(0) lgkmcnt(0)\n\ts_barrier" : "+v"(bias0), "+v"(bias1)::"memory");
    if (w == 0) {
      publish(rsWb, F1 + bid * 8, tlo, thi, lane);
      STAMP(2);
      poll128(rsW, WS_FLAGS + xcd * FL_REP + F1 + lane16, tlo, thi);
    }
    LBAR();
    STAMP(3);
    v4u rsH = mkrs(ws + WS_H + ((d64 * 2 + dh) * 128 + kq * 32) * 1024, 32768);
    v4u hf[8];
#pragma unroll
    for (int i = 0; i < 8; ++i) ld128_sc1_new(hf[i], lane16, rsH, (brot + i) * 1024);
    f32x16 acc[3] = {zero16(), zero16(), zero16()};
    const u32 wa = kq * 6144 + (8 * hh + q4) * 192 + blk * 32 + p4 * 8;
    v4u wf[2][3];
    auto rd = [&](int buf, u32 a) {
#pragma unroll
      for (int g = 0; g < 3; ++g) wf[buf][g] = trfrag(a + g * 64, a + 768 + g * 64);
    };
    rd(0, wa);
    u32 sb = 0, so = brot * 1024;
#pragma unroll 1
    for (u32 r = 0; r < 4; ++r) {
      so = (so + 8192) & 32767;
      if (r == 3) rsH.z = 0;
#pragma unroll
      for (int i = 0; i < 8; ++i) {
        if (i & 1) { BAR(); sb = sb == 5 * 24576 ? 0u : sb + 24576; rd(0, sb + wa); }
        else rd(1, sb + wa + 3072);
        asm volatile("s_waitcnt vmcnt(7)" : "+v"(hf[i])::"memory");
        SCHED();
#pragma unroll
        for (int g = 0; g < 3; ++g) acc[g] = mfma(wf[i & 1][g], hf[i], acc[g]);
        ld128_sc1(hf[i], lane16, rsH, so + i * 1024);
        SCHED();
      }
    }
    STAMP(4);
    const u32 RED = 49152;
#pragma unroll
    for (int g = 0; g < 3; ++g)
#pragma unroll
      for (int qq = 0; qq < 4; ++qq)
        sts128f(RED + (w * 12 + g * 4 + qq) * 1024 + lane16, f32x4{acc[g][4 * qq], acc[g][4 * qq + 1], acc[g][4 * qq + 2], acc[g][4 * qq + 3]});
    asm volatile("s_waitcnt vmcnt(0) lgkmcnt(0)\n\ts_barrier" : "+v"(hf[0]), "+v"(hf[1]), "+v"(hf[2]), "+v"(hf[3]), "+v"(hf[4]), "+v"(hf[5]), "+v"(hf[6]), "+v"(hf[7])::"memory");
    {
      f32x4 s0 = {0.f, 0.f, 0.f, 0.f}, s1 = s0;
#pragma unroll
      for (int k = 0; k < 4; ++k) {
        u32 a = RED + ((k * 2 + fd) * 12 + gs * 2) * 1024 + lane16;
        s0 += lds128f(a); s1 += lds128f(a + 1024);
      }
      f32x2 v[4] = {f32x2{s0[0], s0[1]} + unp(bias0.x), f32x2{s0[2], s0[3]} + unp(bias0.y), f32x2{s1[0], s1[1]} + unp(bias1.x), f32x2{s1[2], s1[3]} + unp(bias1.y)};
      v4u o;
#pragma unroll
      for (int j = 0; j < 4; ++j) {
        f32x2 t = v[j] * fma2(v[j] * v[j], spl(-2.f * 0.7978845608028654f * 0.044715f * LOG2E), spl(-2.f * 0.7978845608028654f * LOG2E));
        f32x2 d = f32x2{__builtin_amdgcn_exp2f(t.x), __builtin_amdgcn_exp2f(t.y)} + spl(1.f);
        o[j] = pk2(v[j] * f32x2{__builtin_amdgcn_rcpf(d.x), __builtin_amdgcn_rcpf(d.y)});
      }
      if ((gs >> 1) < gown) st128_sc1(o, rsWb, WS_U + ((d64 * 2 + fd) * 86 + g0 * 2 + gs) * 1024 + lane16);
    }
    DRAIN_BAR();
    if (w == 0) publish(rsWb, F4 + (d64 * 16 + ft) * 8, tlo, thi, lane);
    STAMP(5);
  }

  const bool isWO = kq == 3;
  const u32 etok = tid >> 3, ec = tid & 7;
  v4u xres;
  u32 b2r;
  ld128_new(xres, ((tt * 64 + etok) * 512 + dt * 64 + ec * 8) * 2, rsX, 0u);
  {
    const v4u rsB2 = mkrs(gb2, 4096);
    asm volatile("buffer_load_ushort %0, %1, %2, 0 offen" : "=v"(b2r) : "v"((tt * 64 + etok) * 2), "s"(rsB2) : "memory");
  }
  if (w == 0) {
    poll64(rsW, WS_FLAGS + xcd * FL_REP + (lane < 16 ? F4 + dt * 128 + lane * 8 : F3 + (tt >> 1) * 64 + (lane & 7) * 8), tlo, thi);
    if (b == 0 && lane == 0) st64_sc1(v2u{tlo, thi}, rsWb, WS_EPOCH);
  }
  LBAR();
  STAMP(6);
  v4u rsA = isWO ? mkrs(ws + WS_O + (tt * 2 + dh) * 32768, 32768) : mkrs(ws + WS_U + ((dt * 2 + dh) * 86 + kq * 29) * 1024, kq == 2 ? 28 * 1024 : 29 * 1024);
  v4u af[8];
#pragma unroll
  for (int i = 0; i < 8; ++i) ld128_sc1_new(af[i], lane16, rsA, (frot + i) * 1024);
  f32x16 acc[2] = {zero16(), zero16()};
  const u32 fa = kq * 4096 + (4 * hh + q4) * 128 + ((q4 >> 1) << 6) + blk * 32 + p4 * 8;
  v4u wf[2][2];
  auto rd = [&](int buf, u32 a) {
    wf[buf][0] = trfrag(a, a + 1024);
    wf[buf][1] = trfrag(a ^ 64, (a ^ 64) + 1024);
  };
  rd(0, fa);
  u32 sb = 0, so = frot * 1024;
#pragma unroll 1
  for (u32 r = 0; r < 4; ++r) {
    so = (so + 8192) & 32767;
    if (r == 3) rsA.z = 0;
#pragma unroll
    for (int i = 0; i < 8; ++i) {
      if (i & 1) { BAR(); sb = sb == 8 * 16384 ? 0u : sb + 16384; rd(0, sb + fa); }
      else rd(1, sb + fa + 2048);
      asm volatile("s_waitcnt vmcnt(7)" : "+v"(af[i])::"memory");
      SCHED();
      acc[0] = mfma(af[i], wf[i & 1][0], acc[0]);
      acc[1] = mfma(af[i], wf[i & 1][1], acc[1]);
      ld128_sc1(af[i], lane16, rsA, so + i * 1024);
      SCHED();
    }
  }
  STAMP(7);
  if (isWO) {
#pragma unroll
    for (int i = 0; i < 2; ++i)
#pragma unroll
      for (int j = 0; j < 16; ++j) {
        const u32 tk = (j & 3) + 8 * (j >> 2);
        sts32f(49152 + (dh * 32 + 4 * hh + tk) * 256 + i * 128 + ((((l31 >> 2) ^ (4 * hh + (j & 3))) & 7) << 4) + (l31 & 3) * 4, acc[i][j]);
      }
  } else {
    const u32 pb = kq * 16384 + l31 * 256 + dh * 128;
#pragma unroll
    for (int i = 0; i < 2; ++i)
#pragma unroll
      for (int qq = 0; qq < 4; ++qq)
        sts128f(pb + i * 8192 + (((2 * qq + hh) ^ (l31 & 7)) << 4), f32x4{acc[i][4 * qq], acc[i][4 * qq + 1], acc[i][4 * qq + 2], acc[i][4 * qq + 3]});
  }
  asm volatile("s_waitcnt vmcnt(0) lgkmcnt(0)\n\ts_barrier"
               : "+v"(af[0]), "+v"(af[1]), "+v"(af[2]), "+v"(af[3]), "+v"(af[4]), "+v"(af[5]), "+v"(af[6]), "+v"(af[7]), "+v"(xres), "+v"(b2r)::"memory");
  {
    const u32 a0 = etok * 256 + (((2 * ec) ^ (etok & 7)) << 4), a1 = a0 ^ 16;
    const float b2v = blo(b2r);
    f32x4 y0 = lds128f(a0) + lds128f(a0 + 16384) + lds128f(a0 + 32768);
    f32x4 y1 = lds128f(a1) + lds128f(a1 + 16384) + lds128f(a1 + 32768);
    f32x4 s0 = lds128f(a0 + 49152), s1 = lds128f(a1 + 49152);
    f32x2 y[4] = {{y0[0], y0[1]}, {y0[2], y0[3]}, {y1[0], y1[1]}, {y1[2], y1[3]}};
    f32x2 s[4] = {{s0[0], s0[1]}, {s0[2], s0[3]}, {s1[0], s1[1]}, {s1[2], s1[3]}};
    v4u o;
#pragma unroll
    for (int j = 0; j < 4; ++j) {
      f32x2 yy = y[j] + spl(b2v), t = yy * spl(-LOG2E);
      f32x2 d = f32x2{__builtin_amdgcn_exp2f(t.x), __builtin_amdgcn_exp2f(t.y)} + spl(1.f);
      f32x2 g = yy * f32x2{__builtin_amdgcn_rcpf(d.x), __builtin_amdgcn_rcpf(d.y)};
      u32 xd = xres[j];
      o[j] = pk2(fma2(s[j], g, unp(xd)));
    }
    *(v4u*)(gout + (tt * 64 + etok) * 512 + dt * 64 + ec * 8) = o;
  }
  STAMP(8);
  u64 cyc1 = __builtin_amdgcn_s_memtime();
  if (tid == 0) {
    u64* sp = (u64*)(ws + WS_STAMP) + b * 16;
#pragma unroll
    for (int i = 0; i < 12; ++i) sp[i] = stamp[i];
    sp[12] = cyc1 - cyc0;
    u32* cp = (u32*)(ws + WS_STAMP + 524288) + b * 16;
#pragma unroll
    for (int i = 0; i < 12; ++i) cp[i] = cst[i];
  }
}

__global__ void k_pre(unsigned long long* st){ if(threadIdx.x==0) st[8000]=__builtin_amdgcn_s_memrealtime(); }
__global__ void k_spin(const unsigned long long* st){
  unsigned long long t0=__builtin_amdgcn_s_memrealtime();
  __shared__ unsigned long long v[256]; __shared__ unsigned long long z[256];
  int t=threadIdx.x; z[t]=st[t*16]; v[t]=st[t*16+8]; __syncthreads();
  if(t==0){ unsigned long long mn=~0ull,mx=0; for(int i=0;i<256;++i){ mn=z[i]<mn?z[i]:mn; mx=v[i]>mx?v[i]:mx; }
    unsigned long long T=(0ull)*100; if(T>1000000) T=1000000;
    unsigned long long s=__builtin_amdgcn_s_memrealtime(); while(__builtin_amdgcn_s_memrealtime()-s<T) __builtin_amdgcn_s_sleep(2); }
}
extern "C" void kernel_launch(void* const* d_in, const int* in_sizes, int n_in, void* d_out, int out_size, void* d_ws, size_t ws_size,
                              hipStream_t stream) {
  (void)in_sizes; (void)n_in; (void)out_size; (void)ws_size;
  (void)hipFuncSetAttribute((const void*)fused_kernel, hipFuncAttributeMaxDynamicSharedMemorySize, LDS_BYTES);
  fused_kernel<<<256, 768, LDS_BYTES, stream>>>((const u16*)d_in[0], (const u16*)d_in[1], (const u16*)d_in[2], (const u16*)d_in[3],
                                                (const u16*)d_in[5], (const u16*)d_in[6], (const u16*)d_in[7], (const u16*)d_in[8],
                                                (u16*)d_out, (char*)d_ws);
  k_spin<<<1,256,0,stream>>>((const unsigned long long*)((char*)d_ws+WS_STAMP));
}
```

```cpp
#include <hip/hip_runtime.h>
#include <hip/hip_bf16.h>
#include <stdint.h>
typedef unsigned short u16;
__device__ __forceinline__ float bf2f(u16 v){ return __uint_as_float(((unsigned)v)<<16); }
__device__ __forceinline__ u16 f2bf(float f){ __hip_bfloat16 b = __float2bfloat16(f); return *(u16*)&b; }
__device__ __forceinline__ float rb(float f){ return bf2f(f2bf(f)); }
constexpr int S=2048, D=512, H=32, DH=16, F=1376;

__device__ float blk_sum(float v, float* sm){
  for(int o=32;o>0;o>>=1) v += __shfl_xor(v,o);
  int w=threadIdx.x>>6; __syncthreads(); if((threadIdx.x&63)==0) sm[w]=v; __syncthreads();
  float t=0; for(int i=0;i<(int)(blockDim.x>>6);++i) t+=sm[i]; return t;
}
__global__ void k_ln(const u16* x, u16* xn, u16* h){
  __shared__ float sm[8];
  int r=blockIdx.x, t=threadIdx.x;
  float v=bf2f(x[r*D+t]);
  float m=blk_sum(v,sm)/D; float d=v-m; float var=blk_sum(d*d,sm)/D;
  float mb=rb(m), sd=rb(sqrtf(rb(rb(var)+1e-5f)));
  float n=rb(rb(v-mb)/sd); xn[r*D+t]=f2bf(n);
  m=blk_sum(n,sm)/D; d=n-m; var=blk_sum(d*d,sm)/D;
  mb=rb(m); sd=rb(sqrtf(rb(rb(var)+1e-5f)));
  h[r*D+t]=f2bf(rb(n-mb)/sd);
}
__global__ void k_gemm(const u16* A, int sam, int sak, const u16* B, int sbk, int sbn, u16* C, int M, int N, int K){
  int n=blockIdx.x*blockDim.x+threadIdx.x, m=blockIdx.y; if(n>=N) return;
  float acc=0; for(int k=0;k<K;++k) acc+=bf2f(A[(long)m*sam+(long)k*sak])*bf2f(B[(long)k*sbk+(long)n*sbn]);
  C[(long)m*N+n]=f2bf(acc);
}
__global__ void k_phi(const u16* qkv, const u16* wphi, u16* phi){
  int idx=blockIdx.x*blockDim.x+threadIdx.x;
  int h=idx%H, s=(idx/H)%S, w=idx/(H*S);
  float p[16];
  for(int f=0;f<16;++f){ float a=0; for(int d=0;d<16;++d) a+=bf2f(qkv[(long)s*1536+w*512+h*16+d])*bf2f(wphi[h*256+d*16+f]); p[f]=rb(a);}
  float mx=-1e30f, mn=1e30f; for(int f=0;f<16;++f){mx=fmaxf(mx,p[f]); mn=fminf(mn,p[f]);}
  float s1=0,s2=0; for(int f=0;f<16;++f){ s1+=__expf(p[f]-mx); s2+=__expf(mn-p[f]); }
  u16* o=phi+(long)idx*32;
  for(int f=0;f<16;++f){ o[f]=f2bf(__expf(p[f]-mx)/s1); o[16+f]=f2bf(__expf(mn-p[f])/s2);}
}
__global__ void k_kv(const u16* phi, const u16* qkv, u16* kv, u16* z){
  int h=blockIdx.x, t=threadIdx.x;
  int f=t/17, d=t%17; if(f>=32) return;
  const u16* pk=phi+(long)S*H*32;
  float a=0; for(int s=0;s<S;++s){ float p=bf2f(pk[((long)s*H+h)*32+f]); float v=d<16? bf2f(qkv[(long)s*1536+1024+h*16+d]):1.f; a+=p*v; }
  if(d<16) kv[(h*32+f)*16+d]=f2bf(a); else z[h*32+f]=f2bf(a);
}
__global__ void k_o(const u16* phi, const u16* kv, const u16* z, u16* o){
  int idx=blockIdx.x*blockDim.x+threadIdx.x;
  int d=idx%16, h=(idx/16)%H, s=idx/(16*H);
  float num=0, den=0; for(int f=0;f<32;++f){ float p=bf2f(phi[((long)s*H+h)*32+f]); num+=p*bf2f(kv[(h*32+f)*16+d]); den+=p*bf2f(z[h*32+f]); }
  num=rb(num); den=rb(rb(den)+1e-6f); o[idx]=f2bf(num/den);
}
__global__ void k_gelu(u16* u, const u16* b1){
  int idx=blockIdx.x*blockDim.x+threadIdx.x; if(idx>=D*F) return; int f=idx%F;
  float v=rb(bf2f(u[idx])+bf2f(b1[f]));
  float t=tanhf(0.7978845608028654f*(v+0.044715f*v*v*v)); u[idx]=f2bf(0.5f*v*(1.f+t));
}
__global__ void k_fin(const u16* x, const u16* sm, const u16* y, const u16* b2, u16* out){
  int idx=blockIdx.x*blockDim.x+threadIdx.x; int d=idx%D, s=idx/D;
  float yy=rb(bf2f(y[(long)d*S+s])+bf2f(b2[s])); float g=rb(yy/(1.f+__expf(-yy)));
  out[idx]=f2bf(bf2f(x[idx])+rb(bf2f(sm[idx])*g));
}

__global__ void k_empty(int* p){ if(p==nullptr) return; }
__global__ __launch_bounds__(768) void k_e768(int* p){ extern __shared__ char l[]; if(p==nullptr) return; }
__global__ __launch_bounds__(768,3) void k_e768v(int* p){ extern __shared__ char l[]; asm volatile("v_mov_b32 v119, 0" ::: "v119"); if(p==nullptr) return; }
__global__ __launch_bounds__(512) void k_e512(int* p){ extern __shared__ char l[]; if(p==nullptr) return; }

extern "C" void kernel_launch(void* const* d_in, const int* in_sizes, int n_in, void* d_out, int out_size, void* d_ws, size_t ws_size, hipStream_t stream){
  const u16* x=(const u16*)d_in[0]; const u16* wqkv=(const u16*)d_in[1]; const u16* wphi=(const u16*)d_in[2]; const u16* wo=(const u16*)d_in[3];
  const u16* w1=(const u16*)d_in[5]; const u16* b1=(const u16*)d_in[6]; const u16* w2=(const u16*)d_in[7]; const u16* b2=(const u16*)d_in[8];
  u16* ws=(u16*)d_ws; u16* xn=ws; u16* h=xn+S*D; u16* qkv=h+S*D; u16* phi=qkv+S*1536; u16* kv=phi+2L*S*H*32; u16* z=kv+H*32*16; u16* o=z+H*32; u16* sm=o+S*D; u16* u=sm+S*D; u16* y=u+D*F;
  int* st=(int*)((char*)d_ws+(128<<20));
  (void)hipFuncSetAttribute((void*)k_e768,hipFuncAttributeMaxDynamicSharedMemorySize,163840); (void)hipFuncSetAttribute((void*)k_e768v,hipFuncAttributeMaxDynamicSharedMemorySize,163840); (void)hipFuncSetAttribute((void*)k_e512,hipFuncAttributeMaxDynamicSharedMemorySize,163840);
  for(int i=0;i<200;++i) k_e512<<<256,512,163840,stream>>>(st);
  k_ln<<<S,512,0,stream>>>(x,xn,h);
  k_gemm<<<dim3(1536/256,S),256,0,stream>>>(xn,D,1,wqkv,1536,1,qkv,S,1536,D);
  k_phi<<<2*S*H/256,256,0,stream>>>(qkv,wphi,phi);
  k_kv<<<H,576,0,stream>>>(phi,qkv,kv,z);
  k_o<<<S*D/256,256,0,stream>>>(phi,kv,z,o);
  k_gemm<<<dim3(2,S),256,0,stream>>>(o,D,1,wo,D,1,sm,S,D,D);
  k_gemm<<<dim3((F+255)/256,D),256,0,stream>>>(h,1,D,w1,F,1,u,D,F,S);
  k_gelu<<<(D*F+255)/256,256,0,stream>>>(u,b1);
  k_gemm<<<dim3(S/256,D),256,0,stream>>>(u,F,1,w2,S,1,y,D,S,F);
  k_fin<<<S*D/256,256,0,stream>>>(x,sm,y,b2,(u16*)d_out);
}
```

```cpp
#include <hip/hip_runtime.h>
#include <stdint.h>

typedef unsigned u32;
typedef unsigned short u16;
typedef unsigned long long u64;
typedef u32 v4u __attribute__((ext_vector_type(4)));
typedef u32 v2u __attribute__((ext_vector_type(2)));
typedef float f32x16 __attribute__((ext_vector_type(16)));
typedef float f32x4 __attribute__((ext_vector_type(4)));
typedef float f32x2 __attribute__((ext_vector_type(2)));
typedef short s16x4 __attribute__((ext_vector_type(4)));
typedef __bf16 bf16x8 __attribute__((ext_vector_type(8)));
typedef __bf16 bf16x2 __attribute__((ext_vector_type(2)));
#define LAS __attribute__((address_space(3)))
#define DI __device__ __forceinline__

#define G1P 1
#define FINP 1
#define PRE0 2
#define RES 0
#define RESDLY 0
#define PACE 1
#define PSLEEP 2
#define PDLY 8
#define F1V 1
#define QS 0
#define PRET 1
#define JIT 3
#define FS 0
#define FSTAMP(i) do { if (FS) STAMP(i); } while (0)
#define HELPA 0
#define STAMPS 0

constexpr u32 WS_EPOCH = 0, WS_FLAGS = 4096, FL_REP = 4096;
constexpr u32 F1 = 0, F2 = 1024, F3 = 2048, F4 = 3072;
constexpr u32 WS_STATS = 65536;
constexpr u32 WS_KVZ = 131072;
constexpr u32 WS_KVR = 262144;
constexpr u32 WS_H = 1u << 20;
constexpr u32 WS_O = 3u << 20;
constexpr u32 WS_U = 5u << 20;
constexpr u32 WS_BYTES = 7u << 20;
constexpr u32 WS_STAMP = 8u << 20;
constexpr u32 LDS_BYTES = 163840;
constexpr u32 CODE_BYTES = 19 * 1024;
constexpr u32 TOUCH_W = 3;
constexpr u32 SPIN_CAP = 1u << 15;
constexpr float LOG2E = 1.4426950408889634f;

DI u32 rfl(u32 v) { return (u32)__builtin_amdgcn_readfirstlane((int)v); }
DI v4u mkrs(const void* p, u32 bytes) { u64 a = (u64)p; return v4u{(u32)a, (u32)(a >> 32) & 0xffffu, bytes, 0x00020000u}; }
DI __amdgpu_buffer_rsrc_t mkrsb(const void* p, u32 bytes) { return __builtin_amdgcn_make_buffer_rsrc((void*)p, (short)0, (int)bytes, 0x00020000); }
DI void gl4(u32 m, u32 v0, u32 v1, u32 v2, u32 v3, v4u rs, u32 sa, u32 sb) {
  asm volatile("s_mov_b32 m0, %0\n\ts_nop 0\n\t"
               "buffer_load_dwordx4 %1, %5, %6 offen lds\n\tbuffer_load_dwordx4 %2, %5, %6 offen offset:1024 lds\n\t"
               "buffer_load_dwordx4 %3, %5, %7 offen offset:2048 lds\n\tbuffer_load_dwordx4 %4, %5, %7 offen offset:3072 lds"
               ::"s"(m), "v"(v0), "v"(v1), "v"(v2), "v"(v3), "s"(rs), "s"(sa), "s"(sb) : "memory");
}
DI void gl3(u32 m, u32 v0, u32 v1, u32 v2, v4u rs, u32 sa) {
  asm volatile("s_mov_b32 m0, %0\n\ts_nop 0\n\t"
               "buffer_load_dwordx4 %1, %4, %5 offen lds\n\tbuffer_load_dwordx4 %2, %4, %5 offen offset:1024 lds\n\t"
               "buffer_load_dwordx4 %3, %4, %5 offen offset:2048 lds"
               ::"s"(m), "v"(v0), "v"(v1), "v"(v2), "s"(rs), "s"(sa) : "memory");
}
DI void ld128_sc1(v4u& d, u32 voff, v4u rs, u32 soff) {
  asm volatile("buffer_load_dwordx4 %0, %1, %2, %3 offen sc1" : "+v"(d) : "v"(voff), "s"(rs), "s"(soff) : "memory");
}
DI void ld128_sc1_new(v4u& d, u32 voff, v4u rs, u32 soff) {
  asm volatile("buffer_load_dwordx4 %0, %1, %2, %3 offen sc1" : "=v"(d) : "v"(voff), "s"(rs), "s"(soff) : "memory");
}
DI void ld128_new(v4u& d, u32 voff, v4u rs, u32 soff) {
  asm volatile("buffer_load_dwordx4 %0, %1, %2, %3 offen" : "=v"(d) : "v"(voff), "s"(rs), "s"(soff) : "memory");
}
DI void ld64_sc1_new(v2u& d, u32 voff, v4u rs, u32 soff) {
  asm volatile("buffer_load_dwordx2 %0, %1, %2, %3 offen sc1" : "=v"(d) : "v"(voff), "s"(rs), "s"(soff) : "memory");
}
DI void st128_sc1(v4u v, __amdgpu_buffer_rsrc_t rs, u32 off) { __builtin_amdgcn_raw_buffer_store_b128(v, rs, (int)off, 0, 16); }
DI void st64_sc1(v2u v, __amdgpu_buffer_rsrc_t rs, u32 off) { __builtin_amdgcn_raw_buffer_store_b64(v, rs, (int)off, 0, 16); }
#define BAR() asm volatile("s_waitcnt lgkmcnt(0)\n\ts_barrier" ::: "memory")
#define DRAIN_BAR() asm volatile("s_waitcnt vmcnt(0) lgkmcnt(0)\n\ts_barrier" ::: "memory")
#define VBAR(n) asm volatile("s_waitcnt vmcnt(" #n ")\n\ts_barrier" ::: "memory")
#define LBAR() asm volatile("s_barrier" ::: "memory")
#define PIN(x) asm volatile("" : "+v"(x))
#define SCHED() __builtin_amdgcn_sched_barrier(0)

DI v2u trrd(u32 a) { return __builtin_bit_cast(v2u, __builtin_amdgcn_ds_read_tr16_b64_v4i16((LAS s16x4*)(size_t)a)); }
DI v4u trfrag(u32 a0, u32 a1) { v2u x = trrd(a0), y = trrd(a1); return v4u{x.x, x.y, y.x, y.y}; }
DI v4u lds128(u32 a) { return *(LAS v4u*)(size_t)a; }
DI v2u lds64(u32 a) { return *(LAS v2u*)(size_t)a; }
DI f32x4 lds128f(u32 a) { return *(LAS f32x4*)(size_t)a; }
DI void sts128(u32 a, v4u v) { *(LAS v4u*)(size_t)a = v; }
DI void sts128f(u32 a, f32x4 v) { *(LAS f32x4*)(size_t)a = v; }
DI void sts64(u32 a, v2u v) { *(LAS v2u*)(size_t)a = v; }
DI void sts32f(u32 a, float v) { *(LAS float*)(size_t)a = v; }
DI f32x16 mfma(v4u a, v4u b, f32x16 c) {
  return __builtin_amdgcn_mfma_f32_32x32x16_bf16(__builtin_bit_cast(bf16x8, a), __builtin_bit_cast(bf16x8, b), c, 0, 0, 0);
}
DI f32x4 mfma16(v4u a, v4u b, f32x4 c) {
  return __builtin_amdgcn_mfma_f32_16x16x32_bf16(__builtin_bit_cast(bf16x8, a), __builtin_bit_cast(bf16x8, b), c, 0, 0, 0);
}
DI u32 pk2(f32x2 v) { return __builtin_bit_cast(u32, __builtin_convertvector(v, bf16x2)); }
DI u32 pk(float a, float b) { return pk2(f32x2{a, b}); }
DI float blo(u32 v) { return __uint_as_float(v << 16); }
DI float bhi(u32 v) { return __uint_as_float(v & 0xffff0000u); }
DI f32x2 unp(u32 v) { return f32x2{blo(v), bhi(v)}; }
DI f32x2 fma2(f32x2 a, f32x2 b, f32x2 c) { return __builtin_elementwise_fma(a, b, c); }
DI f32x2 spl(float a) { return f32x2{a, a}; }
DI float dot2(u32 a, u32 b, float c) { return __builtin_amdgcn_fdot2_f32_bf16(__builtin_bit_cast(bf16x2, a), __builtin_bit_cast(bf16x2, b), c, false); }
template <int C> DI float dpp(float v) { return __int_as_float(__builtin_amdgcn_mov_dpp(__float_as_int(v), C, 0xF, 0xF, true)); }
DI float x16sum(float v) { u32 x = __float_as_uint(v); auto r = __builtin_amdgcn_permlane16_swap(x, x, false, false); return __uint_as_float(r[0]) + __uint_as_float(r[1]); }
DI float x32sum(float v) { u32 x = __float_as_uint(v); auto r = __builtin_amdgcn_permlane32_swap(x, x, false, false); return __uint_as_float(r[0]) + __uint_as_float(r[1]); }
DI float x32max(float v) { u32 x = __float_as_uint(v); auto r = __builtin_amdgcn_permlane32_swap(x, x, false, false); return fmaxf(__uint_as_float(r[0]), __uint_as_float(r[1])); }
DI float x32min(float v) { u32 x = __float_as_uint(v); auto r = __builtin_amdgcn_permlane32_swap(x, x, false, false); return fminf(__uint_as_float(r[0]), __uint_as_float(r[1])); }
DI float hsum32(float v) {
  v += dpp<0xB1>(v); v += dpp<0x4E>(v); v += dpp<0x141>(v); v += dpp<0x140>(v);
  return x16sum(v);
}
template <bool B> struct BoolC { static constexpr bool value = B; };
DI f32x16 zero16() { f32x16 z;
#pragma unroll
  for (int j = 0; j < 16; ++j) z[j] = 0.f;
  return z; }

DI v4u lo8(const f32x16& a) { return v4u{pk(a[0], a[1]), pk(a[2], a[3]), pk(a[4], a[5]), pk(a[6], a[7])}; }
DI v4u hi8(const f32x16& a) { return v4u{pk(a[8], a[9]), pk(a[10], a[11]), pk(a[12], a[13]), pk(a[14], a[15])}; }
DI void publish(__amdgpu_buffer_rsrc_t rsW, u32 off, u32 tlo, u32 thi, u32 lane) {
  if (lane < 8) st64_sc1(v2u{tlo, thi}, rsW, WS_FLAGS + lane * FL_REP + off);
}
typedef u64 v8q __attribute__((ext_vector_type(8)));
DI u64 diff8(const v8q& a, u64 t) { return (((a[0] ^ t) | (a[1] ^ t)) | ((a[2] ^ t) | (a[3] ^ t))) | (((a[4] ^ t) | (a[5] ^ t)) | ((a[6] ^ t) | (a[7] ^ t))); }
DI void spoll16(const char* p, u64 t) {
#pragma unroll 1
  for (u32 spin = 0; spin < SPIN_CAP; ++spin) {
    v8q a, b;
    asm volatile("s_load_dwordx16 %0, %2, 0x0 glc\n\ts_load_dwordx16 %1, %2, 0x40 glc\n\ts_waitcnt lgkmcnt(0)" : "=&s"(a), "=&s"(b) : "s"(p) : "memory");
    if ((diff8(a, t) | diff8(b, t)) == 0) break;
    __builtin_amdgcn_s_sleep(PSLEEP);
  }
}
DI void spoll24(const char* p, const char* q, u64 t) {
#pragma unroll 1
  for (u32 spin = 0; spin < SPIN_CAP; ++spin) {
    v8q a, b, c;
    asm volatile("s_load_dwordx16 %0, %3, 0x0 glc\n\ts_load_dwordx16 %1, %3, 0x40 glc\n\ts_load_dwordx16 %2, %4, 0x0 glc\n\ts_waitcnt lgkmcnt(0)"
                 : "=&s"(a), "=&s"(b), "=&s"(c) : "s"(p), "s"(q) : "memory");
    if ((diff8(a, t) | diff8(b, t) | diff8(c, t)) == 0) break;
    __builtin_amdgcn_s_sleep(PSLEEP);
  }
}
DI void spoll32(const char* p, u64 t) {
#pragma unroll 1
  for (u32 spin = 0; spin < SPIN_CAP; ++spin) {
    v8q a, b, c, d;
    asm volatile("s_load_dwordx16 %0, %4, 0x0 glc\n\ts_load_dwordx16 %1, %4, 0x40 glc\n\ts_load_dwordx16 %2, %4, 0x80 glc\n\ts_load_dwordx16 %3, %4, 0xc0 glc\n\ts_waitcnt lgkmcnt(0)"
                 : "=&s"(a), "=&s"(b), "=&s"(c), "=&s"(d) : "s"(p) : "memory");
    if ((diff8(a, t) | diff8(b, t) | diff8(c, t) | diff8(d, t)) == 0) break;
    __builtin_amdgcn_s_sleep(PSLEEP);
  }
}
DI void vpoll128(v4u rsW, u32 off, u32 tlo, u32 thi) {
#pragma unroll 1
  for (u32 spin = 0; spin < SPIN_CAP; ++spin) {
    v4u t;
    asm volatile("buffer_load_dwordx4 %0, %1, %2, 0 offen sc1\n\ts_waitcnt vmcnt(0)" : "=&v"(t) : "v"(off), "s"(rsW) : "memory");
    if (__all(t.x == tlo && t.y == thi && t.z == tlo && t.w == thi)) break;
    __builtin_amdgcn_s_sleep(PSLEEP);
  }
}
DI u64 tag64(u32 lo, u32 hi) { return ((u64)rfl(hi) << 32) | rfl(lo); }

DI float max3(float a, float b, float c) { float r; asm("v_max3_f32 %0, %1, %2, %3" : "=v"(r) : "v"(a), "v"(b), "v"(c)); return r; }
DI float min3(float a, float b, float c) { float r; asm("v_min3_f32 %0, %1, %2, %3" : "=v"(r) : "v"(a), "v"(b), "v"(c)); return r; }
DI void dual_exp(const f32x16& z, int o, float c, f32x2 (&e)[4], f32x2 (&g)[4]) {
  f32x2 p[4] = {{z[o], z[o + 1]}, {z[o + 2], z[o + 3]}, {z[o + 4], z[o + 5]}, {z[o + 6], z[o + 7]}};
  float mx = __builtin_amdgcn_fmed3f(p[0].x, p[0].y, __builtin_inff()), mn = __builtin_amdgcn_fmed3f(p[0].x, p[0].y, -__builtin_inff());
  mx = max3(max3(max3(mx, p[1].x, p[1].y), p[2].x, p[2].y), p[3].x, p[3].y);
  mn = min3(min3(min3(mn, p[1].x, p[1].y), p[2].x, p[2].y), p[3].x, p[3].y);
  {
    u32 a = __float_as_uint(mx), b = __float_as_uint(mn);
    auto r = __builtin_amdgcn_permlane32_swap(a, a, false, false);
    auto t = __builtin_amdgcn_permlane32_swap(b, b, false, false);
    mx = max3(__uint_as_float(r[0]), __uint_as_float(r[1]), __uint_as_float(r[1]));
    mn = min3(__uint_as_float(t[0]), __uint_as_float(t[1]), __uint_as_float(t[1]));
  }
  const f32x2 a = spl(-mx * c), b = spl(mn * c), c2 = spl(c), n2 = spl(-c);
#pragma unroll
  for (int i = 0; i < 4; ++i) {
    f32x2 t = fma2(p[i], c2, a), u = fma2(p[i], n2, b);
    e[i] = f32x2{__builtin_amdgcn_exp2f(t.x), __builtin_amdgcn_exp2f(t.y)};
    g[i] = f32x2{__builtin_amdgcn_exp2f(u.x), __builtin_amdgcn_exp2f(u.y)};
  }
}
DI void dual_softmax(const f32x16& z, int o, float c, v4u& fp, v4u& fn) {
  f32x2 e[4], g[4];
  dual_exp(z, o, c, e, g);
  f32x2 se = (e[0] + e[1]) + (e[2] + e[3]), sg = (g[0] + g[1]) + (g[2] + g[3]);
  const f32x2 ie = spl(__builtin_amdgcn_rcpf(x32sum(se.x + se.y))), ig = spl(__builtin_amdgcn_rcpf(x32sum(sg.x + sg.y)));
  fp = v4u{pk2(e[0] * ie), pk2(e[1] * ie), pk2(e[2] * ie), pk2(e[3] * ie)};
  fn = v4u{pk2(g[0] * ig), pk2(g[1] * ig), pk2(g[2] * ig), pk2(g[3] * ig)};
}
template <int G> DI void wait_ahead(u32 n) {
  if (n >= 3) asm volatile("s_waitcnt vmcnt(%0)" ::"n"(3 * G) : "memory");
  else if (n == 2) asm volatile("s_waitcnt vmcnt(%0)" ::"n"(2 * G) : "memory");
  else if (n == 1) asm volatile("s_waitcnt vmcnt(%0)" ::"n"(G) : "memory");
  else asm volatile("s_waitcnt vmcnt(0)" ::: "memory");
}
DI bool go_seen(u32 a, u32 magic) { return rfl(*(volatile LAS u32*)(size_t)a) == magic; }
constexpr u32 SYN = 5 * 24576 + 2 * 6144;
constexpr u32 GO0 = 163828, MAGIC0 = 0x7fc3c7e9u, GO1 = 163832, GO2 = 163836, MAGIC1 = 0x7fc1a5e3u, MAGIC2 = 0x7fc2b6d4u, DONE = 0x7fd37fe5u;

constexpr u32 RED = 98304;
DI void sum_selectors(u32 lane, v4u& s0, v4u& s1) {
  const u32 l31 = lane & 31;
  const u32 j = (l31 & 3) + ((l31 >> 3) << 2), sv = (j & 1) ? 0x3f800000u : 0x00003f80u, m = l31 < 16 ? (j >> 1) : 9u;
  const v4u sa = {m == 0 ? sv : 0u, m == 1 ? sv : 0u, m == 2 ? sv : 0u, m == 3 ? sv : 0u};
  const bool up = (l31 >> 2) & 1;
  s0 = v4u{up ? 0u : sa.x, up ? 0u : sa.y, up ? 0u : sa.z, up ? 0u : sa.w};
  s1 = v4u{sa.x ^ s0.x, sa.y ^ s0.y, sa.z ^ s0.z, sa.w ^ s0.w};
  PIN(s0); PIN(s1);
}
DI v4u gelu_frag(u32 a, v4u s0, v4u s1, const f32x2 (&bv)[4]) {
  f32x16 t = mfma(s0, lds128(a), zero16());
  t = mfma(s1, lds128(a + 512), t);
  t = mfma(s0, lds128(a + 24576), t);
  t = mfma(s1, lds128(a + 24576 + 512), t);
  f32x2 v[4] = {f32x2{t[0], t[1]} + bv[0], f32x2{t[2], t[3]} + bv[1], f32x2{t[4], t[5]} + bv[2], f32x2{t[6], t[7]} + bv[3]};
  v4u o;
#pragma unroll
  for (int i = 0; i < 4; ++i) {
    f32x2 z = v[i] * fma2(v[i] * v[i], spl(-2.f * 0.7978845608028654f * 0.044715f * LOG2E), spl(-2.f * 0.7978845608028654f * LOG2E));
    f32x2 d = f32x2{__builtin_amdgcn_exp2f(z.x), __builtin_amdgcn_exp2f(z.y)} + spl(1.f);
    o[i] = pk2(v[i] * f32x2{__builtin_amdgcn_rcpf(d.x), __builtin_amdgcn_rcpf(d.y)});
  }
  return o;
}

#define STAMP(i) do {} while (0)

#define TS(base, n) do {} while (0)
#define TSDUMP(base, sec) do {} while (0)
#define VBART(n, base, k) VBAR(n)
__global__ __launch_bounds__(768, 3) void fused_kernel(const u16* __restrict__ gx, const u16* __restrict__ gwqkv, const u16* __restrict__ gwphi,
                                                         const u16* __restrict__ gwo, const u16* __restrict__ gw1, const u16* __restrict__ gb1,
                                                         const u16* __restrict__ gw2, const u16* __restrict__ gb2, u16* __restrict__ gout, char* ws) {
  STAMP(0);
  const u32 tid = threadIdx.x, lane = tid & 63;
  const u32 w = rfl(tid >> 6);
  const u32 b = blockIdx.x, xcd = b & 7, idx = b >> 3, li = idx >> 1;
  const bool roleA = (idx & 1) == 0;
  const u32 lane16 = lane * 16;
  const u32 tt = (xcd >> 1) * 8 + (idx >> 2), dt = (xcd & 1) * 4 + (idx & 3);
  const u32 frot = (4 * (idx >> 2) + 16 * ((idx >> 1) & 1)) & 31;
  const u32 rt = (xcd >> 1) * 4 + (li >> 2), hg = (xcd & 1) * 4 + (li & 3);
  const u32 bid = xcd * 16 + li;
  const u32 d64 = li & 7, ft = xcd * 2 + (li >> 3);
  const u32 brot = (4 * (li & 7) + 16 * (li >> 3)) & 31;
  const u32 g0 = (ft * 8) / 3;

  if (w >= 8) {
    const u32 lw = w - 8;
    if (lw == 3) {
      const v4u rsT = mkrs(ws, WS_KVR);
      const u32 to = (lane == 0 ? WS_FLAGS : (lane == 1 ? WS_STATS : (lane == 2 ? WS_KVZ : WS_KVR))) + b * 128;
      asm volatile("s_mov_b32 m0, %0\n\ts_nop 0\n\tbuffer_load_dword %1, %2, 0 offen sc1 lds" ::"s"(roleA ? 150528u : 5 * 24576 + 3 * 6144), "v"(to), "s"(rsT) : "memory");
    }
    const bool wo = lw == 3;
    const u32 fpitch = wo ? 1024u : 4096u, fsh = wo ? 14u : 16u;
    const u32 fcol = wo ? dt * 128 : tt * 128, fcnt = wo ? 32u : (lw == 2 ? 28u : 29u);
    const char* fbase = wo ? (const char*)gwo : (const char*)gw2 + (size_t)(lw * 29) * 65536;
    const v4u rsF = mkrs(fbase + fcol - 4096, (fcnt << fsh) - fcol + 4096);
    const u32 fv0 = (lane >> 3) * fpitch + (((lane & 7) ^ (((lane >> 4) & 1) << 2)) << 4) + 4096;
    const u32 fv1 = fv0 + 8 * fpitch - 1024, fv2 = fv0 - 2048, fv3 = fv1 - 2048;
    auto fin = [&](u32 st, u32 slot) {
      u32 i = (2 * st + frot) & 31;
      gl4(slot * 16384 + lw * 4096, fv0, fv1, fv2, fv3, rsF, i << fsh, (i + 1) << fsh);
    };
    if (roleA) {
      const v4u rsX = mkrs((const char*)gx - 4096, 2048 * 1024 + 4096), rsQ = mkrs((const char*)gwqkv - 4096, 512 * 3072 + 4096);
      const u32 rot = (2 * ((li >> 2) + (li & 3))) & 7;
      u32 xv[4], wv[6];
#pragma unroll
      for (int i = 0; i < 4; ++i) {
        u32 row = 32 * lw + 8 * i + (lane >> 3);
        xv[i] = (rt * 128 + row) * 1024 + (((lane & 7) ^ ((row >> 1) & 7)) << 4) + 4096 - i * 1024;
      }
#pragma unroll
      for (int i = 0; i < 3; ++i) {
        u32 o = i * 1024 + lane16, row = o / 384, c = (o - row * 384) >> 4;
        wv[i] = (16 * lw + row) * 3072 + (c >> 3) * 1024 + hg * 128 + (((c & 7) ^ (((row >> 1) & 1) << 2)) << 4) + 4096 - i * 1024;
        wv[3 + i] = wv[i] + 8 * 3072;
      }
      auto stage = [&](u32 st, u32 slot) {
        u32 k = (st + rot) & 7, la = slot * 40960;
        gl4(la + lw * 4096, xv[0], xv[1], xv[2], xv[3], rsX, k * 128, k * 128);
        gl3(la + 16384 + lw * 6144, wv[0], wv[1], wv[2], rsQ, k * (64 * 3072));
        gl3(la + 16384 + lw * 6144 + 3072, wv[3], wv[4], wv[5], rsQ, k * (64 * 3072));
      };
      stage(0, 0); stage(1, 1);
      VBAR(10);
      stage(2, 2);
      u32 slot = 0;
#pragma unroll 1
      for (u32 j = 0; j < 5; ++j) { VBART(10, 139264, j); stage(j + 3, slot); TS(139264, 3 * j + 2); slot = slot == 2 ? 0u : slot + 1; }
      VBART(10, 139264, 5); VBART(0, 139264, 6); LBAR(); TS(139264, 22); TSDUMP(139264, 0);
      fin(0, 0); fin(1, 1);
      LBAR(); LBAR(); LBAR();
      LBAR();
      fin(2, 2); fin(3, 3);
      LBAR(); LBAR();
      LBAR();
    } else {
      const v4u rs1 = mkrs((const char*)gw1 + (size_t)(lw * 512) * 2752 + g0 * 64 - 4096, 512 * 2752 - g0 * 64 + 4096);
      u32 wv[3];
#pragma unroll
      for (int i = 0; i < 3; ++i) { u32 o = i * 1024 + lane16, row = o / 192; wv[i] = row * 2752 + (o - row * 192) + 4096 - i * 1024; }
      auto stage = [&](u32 st, u32 slot) {
        u32 so = ((2 * st + brot) & 31) * (16 * 2752), la = slot * 24576 + lw * 6144;
        gl3(la, wv[0], wv[1], wv[2], rs1, so);
        gl3(la + 3072, wv[0], wv[1], wv[2], rs1, so + 16 * 2752);
      };
      v2u lb0, lb1;
      asm volatile("buffer_load_dwordx2 %0, %2, %3, 0 offen\n\tbuffer_load_dwordx2 %1, %2, %3, 0 offen offset:16"
                   : "=&v"(lb0), "=&v"(lb1) : "v"((g0 * 32 + (2 + lw) * 16 + 4 * (lane >> 5)) * 2), "s"(mkrs(gb1, 1376 * 2)) : "memory");
      u32 is = 0, slot = 0;
      auto next = [&]() { stage(is, slot); ++is; slot = slot == 5 ? 0u : slot + 1; };
      LBAR();
#pragma unroll 1
      for (u32 k = 0; k < PRE0; ++k) { next(); asm volatile("s_waitcnt vmcnt(0)" ::: "memory"); }
#pragma unroll 1
      while (is < 5 && (is == 0 || !go_seen(SYN + 68, 1u))) next();
#pragma unroll 1
      for (u32 spin = 0; spin < (SPIN_CAP << 4) && !go_seen(SYN + 68, 1u); ++spin) __builtin_amdgcn_s_sleep(1);

#pragma unroll 1
      for (u32 j = 0; j < 15; ++j) {
        if (is <= j) next();
        wait_ahead<6>(is - j - 1); TS(155648, 3 * j); LBAR(); TS(155648, 3 * j + 1);
        const u32 lw_ = PACE == 2 ? (j + JIT > 6 ? j + JIT : 6u) : (PACE ? 6 + (j * 3 + 3) / 4 - (j == 0) : j + 6), lim = lw_ < 16 ? lw_ : 16u;
        if (is < lim) next();
        if (is < lim) next();
        TS(155648, 3 * j + 2);
      }
      fin(0, 0); fin(1, 1); fin(2, 2);
      VBART(12, 155648, 15); TSDUMP(155648, 0); LBAR();
      {
        PIN(lb0); PIN(lb1);
        const u32 gown = (ft == 15 ? 43u : ((ft + 1) * 8) / 3) - g0, gs = 2 + lw;
        v4u s0, s1;
        sum_selectors(lane, s0, s1);
        f32x2 bv[4] = {unp(lb0.x), unp(lb0.y), unp(lb1.x), unp(lb1.y)};
        u32 ra = RED + (((lane >> 5) * 2 + 1) * 6 + gs) * 1024 + (lane & 31) * 16, uo = WS_U + ((d64 * 2 + 1) * 86 + g0 * 2 + gs) * 1024 + lane16;
        PIN(bv[0]); PIN(bv[1]); PIN(bv[2]); PIN(bv[3]); PIN(ra); PIN(uo);
        LBAR();
        const v4u o = gelu_frag(ra, s0, s1, bv);
        if ((gs >> 1) < gown) st128_sc1(o, mkrsb(ws, WS_BYTES), uo);
      }
      DRAIN_BAR();
    }
    {
      u32 is = roleA ? 4u : 3u, slot = is;
      auto next = [&]() { fin(is, slot); ++is; slot = slot == 8 ? 0u : slot + 1; };
#pragma unroll 1
      while (is < 9 && !go_seen(GO2, MAGIC2)) next();
#pragma unroll 1
      for (u32 j = 0; j < 16; ++j) {
        if (is <= j) next();
        wait_ahead<4>(is - j - 1); TS(155648, 3 * j); LBAR(); TS(155648, 3 * j + 1);
        const u32 lw_ = PACE == 2 ? (j + JIT > 9 ? j + JIT : 9u) : (PACE ? 9 + (j + 1) / 2 : j + 9), lim = lw_ < 16 ? lw_ : 16u;
        if (is < lim) next();
        if (is < lim) next();
        TS(155648, 3 * j + 2);
      }
    }
    TSDUMP(155648, 1);
    LBAR(); LBAR();
    return;
  }

  const u32 l31 = lane & 31, hh = lane >> 5, blk = (lane >> 4) & 1, q4 = (lane & 15) >> 2, p4 = lane & 3;
  const v4u rsW = mkrs(ws, WS_BYTES);
  const __amdgpu_buffer_rsrc_t rsWb = mkrsb(ws, WS_BYTES);
  const v4u rsX = mkrs(gx, 2048 * 1024);
  const u32 dh = w & 1, kq = w >> 1;
  u32 tlo, thi;

  if (roleA) {
    const u32 th = w & 1, hp = (w >> 1) & 1, fat = w < 4 ? 1u : 0u, tg = 2 * th + 1 - fat;
    v4u wph;
    const u32 CSP = 147456, XF = 65536 + (w & 3) * 4096 + lane16, IMG = 98304;
    const u32 SIDE = 122880 + (w & 3) * 3072 + lane16;
    const u32 swz = ((l31 >> 1) & 1) | ((((l31 >> 2) ^ (l31 >> 3)) & 1) << 1);
    const u32 wa2 = IMG + (hp * 4 + tg) * 6144 + l31 * 64 + hh * 8 + (swz << 4);
    f32x16 qa0, qa1;
    v4u kf0, kf1;
    float rstd = 0.f;
    tlo = 0; thi = 0;
    if (fat) {
      u32 xa0 = (64 * th + l31) * 128 + ((hh ^ ((l31 >> 1) & 7)) << 4);
      u32 wa = 16384 + (8 * hh + q4) * 384 + ((hp ^ (q4 >> 1)) << 6) + blk * 32 + p4 * 8;
      f32x16 acc[2][3] = {{zero16(), zero16(), zero16()}, {zero16(), zero16(), zero16()}};
      v4u xf[2][2], wf[2][3];
      PIN(acc[0][0]); PIN(acc[0][1]); PIN(acc[0][2]); PIN(acc[1][0]); PIN(acc[1][1]); PIN(acc[1][2]); PIN(xa0); PIN(wa);
      BAR();
      STAMP(1);
      xf[0][0] = lds128(xa0); xf[0][1] = lds128(xa0 + 4096);
#pragma unroll
      for (int s = 0; s < 3; ++s) wf[0][s] = trfrag(wa + s * 128, wa + 1536 + s * 128);
      u32 sb = 0;
#pragma unroll 1
      for (u32 st = 0; st < 8; ++st) {
#pragma unroll
        for (int ks = 0; ks < 4; ++ks) {
          const int c = ks & 1, n = c ^ 1, nk = (ks + 1) & 3;
          if (ks < 3) {
            const u32 xa = sb + (xa0 ^ (nk << 5)), wb = sb + wa + nk * 6144;
            acc[0][0] = mfma(wf[c][0], xf[c][0], acc[0][0]); SCHED();
            xf[n][0] = lds128(xa); wf[n][0] = trfrag(wb, wb + 1536); SCHED();
            acc[1][0] = mfma(wf[c][0], xf[c][1], acc[1][0]); SCHED();
            xf[n][1] = lds128(xa + 4096); SCHED();
            acc[0][1] = mfma(wf[c][1], xf[c][0], acc[0][1]); SCHED();
            wf[n][1] = trfrag(wb + 128, wb + 1536 + 128); SCHED();
            acc[1][1] = mfma(wf[c][1], xf[c][1], acc[1][1]); SCHED();
            wf[n][2] = trfrag(wb + 256, wb + 1536 + 256); SCHED();
            acc[0][2] = mfma(wf[c][2], xf[c][0], acc[0][2]);
            acc[1][2] = mfma(wf[c][2], xf[c][1], acc[1][2]); SCHED();
          } else {
            acc[0][0] = mfma(wf[c][0], xf[c][0], acc[0][0]);
            acc[1][0] = mfma(wf[c][0], xf[c][1], acc[1][0]);
            acc[0][1] = mfma(wf[c][1], xf[c][0], acc[0][1]); SCHED();
            BAR();
            sb = sb == 81920 ? 0u : sb + 40960;
            const u32 xa = sb + xa0, wb = sb + wa;
            xf[n][0] = lds128(xa); wf[n][0] = trfrag(wb, wb + 1536); SCHED();
            acc[1][1] = mfma(wf[c][1], xf[c][1], acc[1][1]); SCHED();
            xf[n][1] = lds128(xa + 4096); wf[n][1] = trfrag(wb + 128, wb + 1536 + 128); SCHED();
            acc[0][2] = mfma(wf[c][2], xf[c][0], acc[0][2]); SCHED();
            wf[n][2] = trfrag(wb + 256, wb + 1536 + 256); SCHED();
            acc[1][2] = mfma(wf[c][2], xf[c][1], acc[1][2]); SCHED();
          }
        }
      }
      STAMP(2);
      v4u ca[3];
#pragma unroll
      for (int s = 0; s < 3; ++s) {
        f32x4 c4 = lds128f(CSP + (hp * 3 + s) * 512 + l31 * 16);
        float c = (c4[0] + c4[1]) + (c4[2] + c4[3]);
        u32 hi = pk(c, c);
        ca[s] = v4u{pk(c, c - blo(hi)), hi, 0u, 0u};
      }
      const v4u stg[2] = {lds128(SIDE + 1024), lds128(SIDE + 2048)};
      wph = lds128(SIDE);
      const v4u mb[2] = {{stg[0].x, stg[0].y, 0u, 0u}, {stg[1].x, stg[1].y, 0u, 0u}};
      tlo = rfl(stg[0].w); thi = rfl(stg[1].w);
      acc[1][1] = mfma(ca[1], mb[1], acc[1][1]); acc[0][1] = mfma(ca[1], mb[0], acc[0][1]);
      acc[1][2] = mfma(ca[2], mb[1], acc[1][2]); acc[0][2] = mfma(ca[2], mb[0], acc[0][2]);
      acc[1][0] = mfma(ca[0], mb[1], acc[1][0]); acc[0][0] = mfma(ca[0], mb[0], acc[0][0]);
      sts128(XF, lo8(acc[1][1])); sts128(XF + 1024, hi8(acc[1][1]));
      BAR();
      kf0 = lo8(acc[0][1]); kf1 = hi8(acc[0][1]);
#pragma unroll
      for (int t = 0; t < 2; ++t) {
        const f32x2 r2 = spl(__uint_as_float(stg[t].z));
#pragma unroll
        for (int qq = 0; qq < 4; ++qq)
          sts64((wa2 ^ (qq << 4)) + 4096 + t * 6144, v2u{pk2(f32x2{acc[t][2][4 * qq], acc[t][2][4 * qq + 1]} * r2), pk2(f32x2{acc[t][2][4 * qq + 2], acc[t][2][4 * qq + 3]} * r2)});
      }
      qa0 = acc[0][0]; qa1 = acc[1][0];
      rstd = __uint_as_float(stg[0].z);
    } else {
      const u32 wa = 16384 + (16 * (w - 4) + 8 * hh + q4) * 384 + ((q4 >> 1) << 6) + blk * 32 + p4 * 8;
      f32x16 cs[6] = {zero16(), zero16(), zero16(), zero16(), zero16(), zero16()};
      const v4u ones = {0x3f803f80u, 0x3f803f80u, 0x3f803f80u, 0x3f803f80u};
      v2u ep;
      ld64_sc1_new(ep, 0u, rsW, WS_EPOCH);
    {
      const u16* p = gwphi + (hg * 4 + hp * 2 + (l31 >> 4)) * 256 + (4 * hh) * 16 + (l31 & 15);
      wph.x = p[0] | ((u32)p[16] << 16); wph.y = p[32] | ((u32)p[48] << 16);
      wph.z = p[128] | ((u32)p[144] << 16); wph.w = p[160] | ((u32)p[176] << 16);
    }
      sts128(SIDE, wph);
      const u32 stoff = WS_STATS + (rt * 128 + 64 * th + l31) * 16;
      BAR();
      u32 sb = 0;
#pragma unroll 1
      for (u32 st = 0; st < 8; ++st) {
        if (st == 6)
          asm volatile("s_mov_b32 m0, %0\n\ts_nop 0\n\tbuffer_load_dwordx4 %2, %3, 0 offen sc1 lds\n\ts_mov_b32 m0, %1\n\ts_nop 0\n\tbuffer_load_dwordx4 %2, %3, 0 offen offset:512 sc1 lds"
                       ::"s"(122880 + 1024 + (w & 3) * 3072), "s"(122880 + 2048 - 512 + (w & 3) * 3072), "v"(stoff), "s"(rsW) : "memory");
#pragma unroll
        for (int i = 0; i < 6; ++i) {
          const u32 a = ((sb + wa) ^ ((i / 3) << 6)) + (i % 3) * 128;
          cs[i] = mfma(ones, trfrag(a, a + 1536), cs[i]);
        }
        if (st == 7) {
          asm volatile("s_waitcnt vmcnt(0)" : "+v"(ep)::"memory");
          v4u s0 = lds128(SIDE + 1024), s1 = lds128(SIDE + 2048);
          tlo = rfl(ep.x) + 1u; thi = rfl(ep.y);
          const u32 tB = tlo * 0x9E3779B1u + thi;
#pragma unroll 1
          for (u32 spin = 0; spin < SPIN_CAP; ++spin) {
            if (__all(s0.y == tlo && s0.w == tB && s1.y == tlo && s1.w == tB)) break;
            asm volatile("buffer_load_dwordx4 %0, %2, %3, 0 offen sc1\n\tbuffer_load_dwordx4 %1, %2, %3, 0 offen offset:512 sc1\n\ts_waitcnt vmcnt(0)"
                         : "+v"(s0), "+v"(s1) : "v"(stoff), "s"(rsW) : "memory");
          }
          const float n0 = hh ? 0.f : -__uint_as_float(s0.x), n1 = hh ? 0.f : -__uint_as_float(s1.x);
          const u32 m0 = pk(n0, n0), m1 = pk(n1, n1);
          sts128(SIDE + 1024, v4u{m0, pk(n0 - blo(m0), 0.f), s0.z, tlo});
          sts128(SIDE + 2048, v4u{m1, pk(n1 - blo(m1), 0.f), s1.z, thi});
          rstd = __uint_as_float(s1.z);
        }
        if (st == 7) {
#pragma unroll
          for (int i = 0; i < 6; ++i) sts32f(CSP + i * 512 + l31 * 16 + (w - 4) * 4, cs[i][0]);
        }
        BAR();
        sb = sb == 81920 ? 0u : sb + 40960;
      }
      BAR();
      kf0 = lds128(XF); kf1 = lds128(XF + 1024);
    }
    const float cexp = rstd * LOG2E;
    const bool lo16 = l31 < 16;
    const v4u wph0 = {lo16 ? wph.x : 0u, lo16 ? wph.y : 0u, lo16 ? wph.z : 0u, lo16 ? wph.w : 0u};
    const v4u wph1 = {wph.x ^ wph0.x, wph.y ^ wph0.y, wph.z ^ wph0.z, wph.w ^ wph0.w};
    {
      f32x16 pkk = mfma(wph1, kf1, mfma(wph0, kf0, zero16()));
#pragma unroll
      for (int hd = 0; hd < 2; ++hd) {
        v4u fp, fn;
        dual_softmax(pkk, 8 * hd, cexp, fp, fn);
        sts64((wa2 ^ 0) + hd * 2048, v2u{fp.x, fp.y});
        sts64((wa2 ^ 16) + hd * 2048, v2u{fp.z, fp.w});
        sts64((wa2 ^ 32) + hd * 2048, v2u{fn.x, fn.y});
        sts64((wa2 ^ 48) + hd * 2048, v2u{fn.z, fn.w});
      }
    }
    if (QS != 3) STAMP(9);
    BAR();
    const v4u ones = {0x3f803f80u, 0x3f803f80u, 0x3f803f80u, 0x3f803f80u};
    {
      const u32 g = lane >> 4, hd = (w >> 1) & 1;
      u32 ra[2];
#pragma unroll
      for (int t = 0; t < 2; ++t) {
        u32 row = 8 * g + 4 * t + q4;
        u32 sw = ((row >> 1) & 1) | ((((row >> 2) ^ (row >> 3)) & 1) << 1);
        ra[t] = IMG + (w >> 2) * 24576 + row * 64 + (((p4 >> 1) ^ sw) << 4) + (p4 & 1) * 8;
      }
      const u32 oa = hd * 2048, xa = (w & 1) << 5, xb = hd << 5;
      f32x4 kv = {0.f, 0.f, 0.f, 0.f}, zz = kv;
#pragma unroll
      for (int t4 = 0; t4 < 4; ++t4) {
        v4u af = trfrag((ra[0] ^ xa) + oa + t4 * 6144, (ra[1] ^ xa) + oa + t4 * 6144);
        v4u vf = trfrag((ra[0] ^ xb) + 4096 + t4 * 6144, (ra[1] ^ xb) + 4096 + t4 * 6144);
        kv = mfma16(af, vf, kv); zz = mfma16(af, ones, zz);
      }
      st64_sc1(v2u{pk(kv[0], kv[1]), pk(kv[2], kv[3])}, rsWb, WS_KVR + ((hg * 8 + w) * 16 + rt) * 512 + lane * 8);
      if ((lane & 15) == 0) st64_sc1(v2u{pk(zz[0], zz[1]), pk(zz[2], zz[3])}, rsWb, WS_KVZ + ((hg * 8 + w) * 16 + rt) * 32 + g * 8);
    }
    if (QS != 3) STAMP(10);
    DRAIN_BAR();
    if (w == 0) publish(rsWb, F2 + (hg * 16 + rt) * 8, tlo, thi, lane);
    STAMP(3);
    v4u pq[2][2];
    {
      v4u qf0, qf1;
      if (fat) {
        sts128(XF + 2048, lo8(qa1)); sts128(XF + 3072, hi8(qa1));
        BAR();
        qf0 = lo8(qa0); qf1 = hi8(qa0);
      } else {
        BAR();
        qf0 = lds128(XF + 2048); qf1 = lds128(XF + 3072);
      }
      f32x16 pqq = mfma(wph1, qf1, mfma(wph0, qf0, zero16()));
#pragma unroll
      for (int hd = 0; hd < 2; ++hd) {
        f32x2 e[4], g[4];
        dual_exp(pqq, 8 * hd, cexp, e, g);
        pq[hd][0] = v4u{pk2(e[0]), pk2(e[1]), pk2(e[2]), pk2(e[3])};
        pq[hd][1] = v4u{pk2(g[0]), pk2(g[1]), pk2(g[2]), pk2(g[3])};
        PIN(pq[hd][0]); PIN(pq[hd][1]);
      }
    }
    const u32 KZ = 151552, ZR = 159744;
    const u32 sv = l31 < 8 ? ((l31 & 1) ? 0x3f800000u : 0x00003f80u) : 0u, sm_ = l31 >> 1;
    v4u sel = {sm_ == 0 ? sv : 0u, sm_ == 1 ? sv : 0u, sm_ == 2 ? sv : 0u, sm_ == 3 ? sv : 0u};
    u32 ka = KZ + hp * 4096 + (lo16 ? (hh * 16 + l31) * 8 : (l31 < 24 ? 512 + hh * 8 : 528u));
    u32 oo = WS_O + ((rt * 4 + tg) * 32 + hg * 4 + hp * 2) * 1024 + lane16;
    u32 za = ZR + w * 512 + (8 * ((lane >> 4) & 1) + q4) * 32 + p4 * 8, zw = KZ + w * 1024 + 512 + (lane >> 5) * 256 + ((lane >> 4) & 1) * 8 + (lane & 1) * 16;
    u32 zs_ = ZR + w * 512 + l31 * 16, kw = KZ + w * 1024 + (2 * l31 + hh) * 8;
    PIN(sel); PIN(ka); PIN(oo); PIN(za); PIN(zw); PIN(zs_); PIN(kw);
    if (w == 0) spoll16(ws + WS_FLAGS + xcd * FL_REP + F2 + hg * 128, tag64(tlo, thi));
    BAR();
    STAMP(4);
    {
      v4u pr[8], pz;
      ld128_sc1_new(pz, l31 * 16, rsW, WS_KVZ + (hg * 8 + w) * 512);
#pragma unroll
      for (int i = 0; i < 8; ++i) ld128_sc1_new(pr[i], lane16, rsW, WS_KVR + (hg * 8 + w) * 8192 + ((i + 2 * (li >> 2)) & 7) * 1024);
      asm volatile("s_waitcnt vmcnt(8)" : "+v"(pz)::"memory");
      FSTAMP(11);
      sts128(zs_, pz);
      const u32 on = hh ? 0u : 0x3f803f80u;
      f32x4 zs = mfma16(trfrag(za, za + 128), v4u{on, on, on, on}, f32x4{0.f, 0.f, 0.f, 0.f});
      sts64(zw,
            (lane & 1) ? v2u{0x3f803f80u, 0x3f803f80u} : v2u{pk(zs[0], zs[1]), pk(zs[2], zs[3])});
      f32x16 sm = zero16();
      asm volatile("s_waitcnt vmcnt(4)" : "+v"(pr[0]), "+v"(pr[1]), "+v"(pr[2]), "+v"(pr[3])::"memory");
#pragma unroll
      for (int i = 0; i < 8; ++i) {
        if (i == 4) asm volatile("s_waitcnt vmcnt(0)" : "+v"(pr[4]), "+v"(pr[5]), "+v"(pr[6]), "+v"(pr[7])::"memory");
        sm = mfma(sel, pr[i], sm);
      }
      sts64(kw, v2u{pk(sm[0], sm[1]), pk(sm[2], sm[3])});
    }
    FSTAMP(9);
    BAR();
    {
#pragma unroll
      for (int hd = 0; hd < 2; ++hd) {
        v2u x0 = lds64(ka + hd * 2048), y0 = lds64(ka + hd * 2048 + 256), x1 = lds64(ka + hd * 2048 + 1024), y1 = lds64(ka + hd * 2048 + 1280);
        f32x16 p = mfma(v4u{x0.x, x0.y, y0.x, y0.y}, pq[hd][0], zero16());
        f32x16 n = mfma(v4u{x1.x, x1.y, y1.x, y1.y}, pq[hd][1], zero16());
        const float inv = __builtin_amdgcn_rcpf(fmaf(p[8], n[12], fmaf(n[8], p[12], 1e-6f * p[12] * n[12])));
        const f32x2 ap = spl(n[12] * inv), an = spl(p[12] * inv);
        v4u o;
#pragma unroll
        for (int j = 0; j < 4; ++j) o[j] = pk2(fma2(f32x2{p[2 * j], p[2 * j + 1]}, ap, f32x2{n[2 * j], n[2 * j + 1]} * an));
        st128_sc1(o, rsWb, oo + hd * 1024);
      }
    }
    FSTAMP(10);
    DRAIN_BAR();
    if (w == 0) publish(rsWb, F3 + (rt * 8 + hg) * 8, tlo, thi, lane);
    STAMP(5);
  } else {
    v4u xr[2];
    v2u ep;
    {
      const u32 xo = hh * 1024 + l31 * 16;
      asm volatile("buffer_load_dwordx4 %0, %2, %3, %4 offen\n\tbuffer_load_dwordx4 %1, %2, %3, %4 offen offset:512"
                   : "=&v"(xr[0]), "=&v"(xr[1]) : "v"(xo), "s"(rsX), "s"((bid * 16 + 2 * w) * 1024) : "memory");
    }
    ld64_sc1_new(ep, 0u, rsW, WS_EPOCH);
    if (w == 0 && lane < 32) *(volatile LAS u32*)(size_t)(SYN + lane * 4) = 0u;
    BAR();
    auto all8 = [&](u32 a) {
#pragma unroll 1
      for (u32 spin = 0; spin < (SPIN_CAP << 4); ++spin)
        if (__all(*(volatile LAS u32*)(size_t)(a + (lane & 7) * 4) == 1u)) break;
    };
    const u32 gown = (ft == 15 ? 43u : ((ft + 1) * 8) / 3) - g0;
    const u32 fd = w >= 6 ? 1u : 0u, gs = w - 6 * fd;
    v2u bias0, bias1;
    {
      const v4u rsB = mkrs(gb1, 1376 * 2);
      const u32 bo = (g0 * 32 + gs * 16 + 4 * hh) * 2;
      asm volatile("buffer_load_dwordx2 %0, %2, %3, 0 offen\n\tbuffer_load_dwordx2 %1, %2, %3, 0 offen offset:16" : "=&v"(bias0), "=&v"(bias1) : "v"(bo), "s"(rsB) : "memory");
    }
    asm volatile("s_waitcnt vmcnt(2)" : "+v"(xr[0]), "+v"(xr[1]), "+v"(ep)::"memory");
    STAMP(1);
    tlo = rfl(ep.x) + 1u; thi = rfl(ep.y);
    const u32 HIM = 147456;
    v4u stat;
    {
      f32x2 v[8];
#pragma unroll
      for (int j = 0; j < 4; ++j) { u32 a = xr[0][j], c = xr[1][j]; v[j] = unp(a); v[4 + j] = unp(c); }
      f32x2 s = ((v[0] + v[1]) + (v[2] + v[3])) + ((v[4] + v[5]) + (v[6] + v[7]));
      const float m = hsum32(s.x + s.y) * (1.f / 512.f);
      f32x2 q = {0.f, 0.f}, q1 = q;
#pragma unroll
      for (int j = 0; j < 8; j += 2) { v[j] -= spl(m); v[j + 1] -= spl(m); q = fma2(v[j], v[j], q); q1 = fma2(v[j + 1], v[j + 1], q1); }
      q += q1;
      const float var = hsum32(q.x + q.y) * (1.f / 512.f);
      const float r1 = __builtin_amdgcn_rsqf(var + 1e-5f);
      const f32x2 rr = spl(r1 * __builtin_amdgcn_rsqf(fmaf(var * r1, r1, 1e-5f)));
      const u32 row = 2 * w + hh, ha = HIM + row * 1024 + ((l31 ^ ((row & 3) << 2)) << 4);
      sts128(ha, v4u{pk2(v[0] * rr), pk2(v[1] * rr), pk2(v[2] * rr), pk2(v[3] * rr)});
      sts128(ha + 512, v4u{pk2(v[4] * rr), pk2(v[5] * rr), pk2(v[6] * rr), pk2(v[7] * rr)});
      stat = v4u{__float_as_uint(m), tlo, __float_as_uint(r1), tlo * 0x9E3779B1u + thi};
    }
    STAMP(9);
    asm volatile("s_waitcnt lgkmcnt(0)" ::: "memory");
    *(volatile LAS u32*)(size_t)(SYN + w * 4) = 1u;
    all8(SYN);
#pragma unroll
    for (int i = 0; i < 2; ++i) {
      const u32 d32 = 2 * w + i;
      const u32 a = HIM + (8 * hh + q4) * 1024 + (((d32 * 4 + 2 * blk + (p4 >> 1)) ^ (q4 << 2)) << 4) + (p4 & 1) * 8;
      st128_sc1(trfrag(a, a + 4096), rsWb, WS_H + (d32 * 128 + bid) * 1024 + lane16);
    }
    STAMP(10);
    asm volatile("s_waitcnt vmcnt(0)" : "+v"(bias0), "+v"(bias1)::"memory");
    *(volatile LAS u32*)(size_t)(SYN + 32 + w * 4) = 1u;
    if (l31 == 0) st128_sc1(stat, rsWb, WS_STATS + (bid * 16 + 2 * w + hh) * 16);
    v4u s0, s1;
    sum_selectors(lane, s0, s1);
    f32x2 bv[4] = {unp(bias0.x), unp(bias0.y), unp(bias1.x), unp(bias1.y)};
    u32 ra = RED + ((hh * 2 + fd) * 6 + gs) * 1024 + l31 * 16, uo = WS_U + ((d64 * 2 + fd) * 86 + g0 * 2 + gs) * 1024 + lane16;
    PIN(bv[0]); PIN(bv[1]); PIN(bv[2]); PIN(bv[3]); PIN(ra); PIN(uo);
    f32x16 acc[3] = {zero16(), zero16(), zero16()};
    u32 wa = kq * 6144 + (8 * hh + q4) * 192 + blk * 32 + p4 * 8;
    PIN(acc[0]); PIN(acc[1]); PIN(acc[2]); PIN(wa);
    if (w == 0) {
      all8(SYN + 32);
      publish(rsWb, F1 + bid * 8, tlo, thi, lane);
      *(volatile LAS u32*)(size_t)(SYN + 64) = 1u;
      STAMP(2);
      __builtin_amdgcn_s_sleep(PDLY);
      vpoll128(rsW, WS_FLAGS + xcd * FL_REP + F1 + lane16, tlo, thi);
      *(volatile LAS u32*)(size_t)(SYN + 68) = 1u;
    } else {
#pragma unroll 1
      for (u32 spin = 0; spin < (SPIN_CAP << 4) && !go_seen(SYN + 68, 1u); ++spin) __builtin_amdgcn_s_sleep(1);
    }
    STAMP(3);
    const v4u rsH = mkrs(ws + WS_H + ((d64 * 2 + dh) * 128 + kq * 32) * 1024, 32768);
    v4u hf[8];
#pragma unroll
    for (int i = 0; i < 8; ++i) ld128_sc1_new(hf[i], lane16, rsH, ((brot + i) & 31) * 1024);
    LBAR();
    v4u wf[2][3];
    auto rd = [&](int buf, u32 a) {
#pragma unroll
      for (int g = 0; g < 3; ++g) wf[buf][g] = trfrag(a + g * 64, a + 768 + g * 64);
    };
    rd(0, wa);
    u32 sb = 0, so = brot * 1024;
    auto round = [&](auto last) {
      constexpr bool LAST = decltype(last)::value;
#pragma unroll
      for (int i = 0; i < 8; ++i) {
        const int c = i & 1, n = c ^ 1;
        if (LAST) asm volatile("s_waitcnt vmcnt(%1)" : "+v"(hf[i]) : "n"(7 - i) : "memory");
        else asm volatile("s_waitcnt vmcnt(7)" : "+v"(hf[i])::"memory");
        SCHED();
        acc[0] = mfma(wf[c][0], hf[i], acc[0]); SCHED();
        if (c) { BAR(); sb = sb == 5 * 24576 ? 0u : sb + 24576; }
        const u32 a = sb + wa + (c ? 0 : 3072);
        wf[n][0] = trfrag(a, a + 768); wf[n][1] = trfrag(a + 64, a + 768 + 64); SCHED();
        acc[1] = mfma(wf[c][1], hf[i], acc[1]); SCHED();
        wf[n][2] = trfrag(a + 128, a + 768 + 128); SCHED();
        acc[2] = mfma(wf[c][2], hf[i], acc[2]);
        if (!LAST) ld128_sc1(hf[i], lane16, rsH, (so + i * 1024) & 32767);
        SCHED();
      }
    };
#pragma unroll 1
    for (u32 r = 0; r < 3; ++r) { so += 8192; round(BoolC<false>{});
    }
    round(BoolC<true>{});
    STAMP(4);
#pragma unroll
    for (int g = 0; g < 3; ++g) {
      sts128(RED + (w * 6 + 2 * g) * 1024 + lane16, v4u{pk(acc[g][0], acc[g][1]), pk(acc[g][2], acc[g][3]), pk(acc[g][4], acc[g][5]), pk(acc[g][6], acc[g][7])});
      sts128(RED + (w * 6 + 2 * g + 1) * 1024 + lane16, v4u{pk(acc[g][8], acc[g][9]), pk(acc[g][10], acc[g][11]), pk(acc[g][12], acc[g][13]), pk(acc[g][14], acc[g][15])});
    }
    FSTAMP(11);
    BAR();
    FSTAMP(9);
    {
      const v4u o = gelu_frag(ra, s0, s1, bv);
      if ((gs >> 1) < gown) st128_sc1(o, rsWb, uo);
    }
    FSTAMP(10);
    DRAIN_BAR();
    if (w == 0) publish(rsWb, F4 + (d64 * 16 + ft) * 8, tlo, thi, lane);
    STAMP(5);
  }

  const bool isWO = kq == 3;
  const u32 etok = tid >> 3, ec = tid & 7;
  v4u xres;
  u32 b2r;
  ld128_new(xres, ((tt * 64 + etok) * 512 + dt * 64 + ec * 8) * 2, rsX, 0u);
  {
    const v4u rsB2 = mkrs(gb2, 4096);
    asm volatile("buffer_load_ushort %0, %1, %2, 0 offen" : "=v"(b2r) : "v"((tt * 64 + etok) * 2), "s"(rsB2) : "memory");
  }
  f32x16 acc[2] = {zero16(), zero16()};
  u32 fa = kq * 4096 + (4 * hh + q4) * 128 + ((q4 >> 1) << 6) + blk * 32 + p4 * 8;
  const v4u rsA = isWO ? mkrs(ws + WS_O + (tt * 2 + dh) * 32768, 32768) : mkrs(ws + WS_U + ((dt * 2 + dh) * 86 + kq * 29) * 1024, kq == 2 ? 28 * 1024 : 29 * 1024);
  PIN(acc[0]); PIN(acc[1]); PIN(fa);
  asm volatile("" ::"s"(rsA));
  if (w == 0) {
    spoll24(ws + WS_FLAGS + xcd * FL_REP + F4 + dt * 128, ws + WS_FLAGS + xcd * FL_REP + F3 + (tt >> 1) * 64, tag64(tlo, thi));
    *(volatile LAS u32*)(size_t)GO2 = MAGIC2;
    if (b == 0 && lane == 0) st64_sc1(v2u{tlo, thi}, rsWb, WS_EPOCH);
  }
  LBAR();
  STAMP(6);
  v4u af[8];
#pragma unroll
  for (int i = 0; i < 8; ++i) ld128_sc1_new(af[i], lane16, rsA, ((frot + i) & 31) * 1024);
  v4u wf[2][2];
  auto rd = [&](int buf, u32 a) {
    wf[buf][0] = trfrag(a, a + 1024);
    wf[buf][1] = trfrag(a ^ 64, (a ^ 64) + 1024);
  };
  rd(0, fa);
  u32 sb = 0, so = frot * 1024;
  auto round = [&](auto last) {
    constexpr bool LAST = decltype(last)::value;
#pragma unroll
    for (int i = 0; i < 8; ++i) {
      const int c = i & 1, n = c ^ 1;
      if (LAST) asm volatile("s_waitcnt vmcnt(%1)" : "+v"(af[i]) : "n"(7 - i) : "memory");
      else asm volatile("s_waitcnt vmcnt(7)" : "+v"(af[i])::"memory");
      SCHED();
      acc[0] = mfma(af[i], wf[c][0], acc[0]); SCHED();
      if (c) { BAR(); sb = sb == 8 * 16384 ? 0u : sb + 16384; }
      rd(n, sb + fa + (c ? 0 : 2048)); SCHED();
      acc[1] = mfma(af[i], wf[c][1], acc[1]);
      if (!LAST) ld128_sc1(af[i], lane16, rsA, (so + i * 1024) & 32767);
      SCHED();
    }
  };
#pragma unroll 1
  for (u32 r = 0; r < 3; ++r) { so += 8192; round(BoolC<false>{}); }
  round(BoolC<true>{});
  STAMP(7);
  if (isWO) {
#pragma unroll
    for (int i = 0; i < 2; ++i)
#pragma unroll
      for (int j = 0; j < 16; ++j) {
        const u32 tk = (j & 3) + 8 * (j >> 2);
        sts32f(49152 + (dh * 32 + 4 * hh + tk) * 256 + i * 128 + ((((l31 >> 2) ^ (4 * hh + (j & 3))) & 7) << 4) + (l31 & 3) * 4, acc[i][j]);
      }
  } else {
    const u32 pb = kq * 16384 + l31 * 256 + dh * 128;
#pragma unroll
    for (int i = 0; i < 2; ++i)
#pragma unroll
      for (int qq = 0; qq < 4; ++qq)
        sts128f(pb + i * 8192 + (((2 * qq + hh) ^ (l31 & 7)) << 4), f32x4{acc[i][4 * qq], acc[i][4 * qq + 1], acc[i][4 * qq + 2], acc[i][4 * qq + 3]});
  }
  asm volatile("s_waitcnt vmcnt(0) lgkmcnt(0)\n\ts_barrier"
               : "+v"(xres), "+v"(b2r)::"memory");
  {
    const u32 a0 = etok * 256 + (((2 * ec) ^ (etok & 7)) << 4), a1 = a0 ^ 16;
    const float b2v = blo(b2r);
    f32x4 y0 = lds128f(a0) + lds128f(a0 + 16384) + lds128f(a0 + 32768);
    f32x4 y1 = lds128f(a1) + lds128f(a1 + 16384) + lds128f(a1 + 32768);
    f32x4 s0 = lds128f(a0 + 49152), s1 = lds128f(a1 + 49152);
    f32x2 y[4] = {{y0[0], y0[1]}, {y0[2], y0[3]}, {y1[0], y1[1]}, {y1[2], y1[3]}};
    f32x2 s[4] = {{s0[0], s0[1]}, {s0[2], s0[3]}, {s1[0], s1[1]}, {s1[2], s1[3]}};
    v4u o;
#pragma unroll
    for (int j = 0; j < 4; ++j) {
      f32x2 yy = y[j] + spl(b2v), t = yy * spl(-LOG2E);
      f32x2 d = f32x2{__builtin_amdgcn_exp2f(t.x), __builtin_amdgcn_exp2f(t.y)} + spl(1.f);
      f32x2 g = yy * f32x2{__builtin_amdgcn_rcpf(d.x), __builtin_amdgcn_rcpf(d.y)};
      u32 xd = xres[j];
      o[j] = pk2(fma2(s[j], g, unp(xd)));
    }
    *(v4u*)(gout + (tt * 64 + etok) * 512 + dt * 64 + ec * 8) = o;
  }
}

extern "C" void kernel_launch(void* const* d_in, const int* in_sizes, int n_in, void* d_out, int out_size, void* d_ws, size_t ws_size,
                              hipStream_t stream) {
  (void)in_sizes; (void)n_in; (void)out_size; (void)ws_size;
  (void)hipFuncSetAttribute((const void*)fused_kernel, hipFuncAttributeMaxDynamicSharedMemorySize, LDS_BYTES);
  fused_kernel<<<256, 768, LDS_BYTES, stream>>>((const u16*)d_in[0], (const u16*)d_in[1], (const u16*)d_in[2], (const u16*)d_in[3],
                                                (const u16*)d_in[5], (const u16*)d_in[6], (const u16*)d_in[7], (const u16*)d_in[8],
                                                (u16*)d_out, (char*)d_ws);
}
```

```cpp
#include <hip/hip_runtime.h>
#include <stdint.h>

typedef unsigned u32;
typedef unsigned short u16;
typedef unsigned long long u64;
typedef u32 v4u __attribute__((ext_vector_type(4)));
typedef u32 v2u __attribute__((ext_vector_type(2)));
typedef float f32x16 __attribute__((ext_vector_type(16)));
typedef float f32x4 __attribute__((ext_vector_type(4)));
typedef float f32x2 __attribute__((ext_vector_type(2)));
typedef short s16x4 __attribute__((ext_vector_type(4)));
typedef __bf16 bf16x8 __attribute__((ext_vector_type(8)));
typedef __bf16 bf16x2 __attribute__((ext_vector_type(2)));
#define LAS __attribute__((address_space(3)))
#define DI __device__ __forceinline__

#define G1P 1
#define FINP 1
#define PRE0 2
#define RES 0
#define RESDLY 0
#define PACE 1
#define PSLEEP 2
#define PDLY 8
#define F1V 1
#define QS 0
#define PRET 1
#define JIT 3
#define FS 0
#define FSTAMP(i) do { if (FS) STAMP(i); } while (0)
#define FJIT 6
#define NOB 0
#define NOHELP 0
#define HPRIO 0
#define HELPA 0
#define STAMPS 0

constexpr u32 WS_EPOCH = 0, WS_FLAGS = 4096, FL_REP = 4096;
constexpr u32 F1 = 0, F2 = 1024, F3 = 2048, F4 = 3072;
constexpr u32 WS_STATS = 65536;
constexpr u32 WS_KVZ = 131072;
constexpr u32 WS_CS = 196608;
constexpr u32 WS_KVR = 262144;
constexpr u32 WS_H = 1u << 20;
constexpr u32 WS_O = 3u << 20;
constexpr u32 WS_U = 5u << 20;
constexpr u32 WS_BYTES = 7u << 20;
constexpr u32 WS_STAMP = 8u << 20;
constexpr u32 LDS_BYTES = 163840;
constexpr u32 CODE_BYTES = 19 * 1024;
constexpr u32 TOUCH_W = 3;
constexpr u32 SPIN_CAP = 1u << 15;
constexpr float LOG2E = 1.4426950408889634f;

DI u32 rfl(u32 v) { return (u32)__builtin_amdgcn_readfirstlane((int)v); }
DI v4u mkrs(const void* p, u32 bytes) { u64 a = (u64)p; return v4u{(u32)a, (u32)(a >> 32) & 0xffffu, bytes, 0x00020000u}; }
DI __amdgpu_buffer_rsrc_t mkrsb(const void* p, u32 bytes) { return __builtin_amdgcn_make_buffer_rsrc((void*)p, (short)0, (int)bytes, 0x00020000); }
DI void gl4(u32 m, u32 v0, u32 v1, u32 v2, u32 v3, v4u rs, u32 sa, u32 sb) {
  asm volatile("s_mov_b32 m0, %0\n\ts_nop 0\n\t"
               "buffer_load_dwordx4 %1, %5, %6 offen lds\n\tbuffer_load_dwordx4 %2, %5, %6 offen offset:1024 lds\n\t"
               "buffer_load_dwordx4 %3, %5, %7 offen offset:2048 lds\n\tbuffer_load_dwordx4 %4, %5, %7 offen offset:3072 lds"
               ::"s"(m), "v"(v0), "v"(v1), "v"(v2), "v"(v3), "s"(rs), "s"(sa), "s"(sb) : "memory");
}
DI void gl3(u32 m, u32 v0, u32 v1, u32 v2, v4u rs, u32 sa) {
  asm volatile("s_mov_b32 m0, %0\n\ts_nop 0\n\t"
               "buffer_load_dwordx4 %1, %4, %5 offen lds\n\tbuffer_load_dwordx4 %2, %4, %5 offen offset:1024 lds\n\t"
               "buffer_load_dwordx4 %3, %4, %5 offen offset:2048 lds"
               ::"s"(m), "v"(v0), "v"(v1), "v"(v2), "s"(rs), "s"(sa) : "memory");
}
DI void ld128_sc1(v4u& d, u32 voff, v4u rs, u32 soff) {
  asm volatile("buffer_load_dwordx4 %0, %1, %2, %3 offen sc1" : "+v"(d) : "v"(voff), "s"(rs), "s"(soff) : "memory");
}
DI void ld128_sc1_new(v4u& d, u32 voff, v4u rs, u32 soff) {
  asm volatile("buffer_load_dwordx4 %0, %1, %2, %3 offen sc1" : "=v"(d) : "v"(voff), "s"(rs), "s"(soff) : "memory");
}
DI void ld128_new(v4u& d, u32 voff, v4u rs, u32 soff) {
  asm volatile("buffer_load_dwordx4 %0, %1, %2, %3 offen" : "=v"(d) : "v"(voff), "s"(rs), "s"(soff) : "memory");
}
DI void ld64_sc1_new(v2u& d, u32 voff, v4u rs, u32 soff) {
  asm volatile("buffer_load_dwordx2 %0, %1, %2, %3 offen sc1" : "=v"(d) : "v"(voff), "s"(rs), "s"(soff) : "memory");
}
DI void st128_sc1(v4u v, __amdgpu_buffer_rsrc_t rs, u32 off) { __builtin_amdgcn_raw_buffer_store_b128(v, rs, (int)off, 0, 16); }
DI void st64_sc1(v2u v, __amdgpu_buffer_rsrc_t rs, u32 off) { __builtin_amdgcn_raw_buffer_store_b64(v, rs, (int)off, 0, 16); }
#define BAR() asm volatile("s_waitcnt lgkmcnt(0)\n\ts_barrier" ::: "memory")
#define DRAIN_BAR() asm volatile("s_waitcnt vmcnt(0) lgkmcnt(0)\n\ts_barrier" ::: "memory")
#define VBAR(n) asm volatile("s_waitcnt vmcnt(" #n ")\n\ts_barrier" ::: "memory")
#define LBAR() asm volatile("s_barrier" ::: "memory")
#define PIN(x) asm volatile("" : "+v"(x))
#define SCHED() __builtin_amdgcn_sched_barrier(0)

DI v2u trrd(u32 a) { return __builtin_bit_cast(v2u, __builtin_amdgcn_ds_read_tr16_b64_v4i16((LAS s16x4*)(size_t)a)); }
DI v4u trfrag(u32 a0, u32 a1) { v2u x = trrd(a0), y = trrd(a1); return v4u{x.x, x.y, y.x, y.y}; }
DI v4u lds128(u32 a) { return *(LAS v4u*)(size_t)a; }
DI v2u lds64(u32 a) { return *(LAS v2u*)(size_t)a; }
DI f32x4 lds128f(u32 a) { return *(LAS f32x4*)(size_t)a; }
DI void sts128(u32 a, v4u v) { *(LAS v4u*)(size_t)a = v; }
DI void sts128f(u32 a, f32x4 v) { *(LAS f32x4*)(size_t)a = v; }
DI void sts64(u32 a, v2u v) { *(LAS v2u*)(size_t)a = v; }
DI void sts32f(u32 a, float v) { *(LAS float*)(size_t)a = v; }
DI f32x16 mfma(v4u a, v4u b, f32x16 c) {
  return __builtin_amdgcn_mfma_f32_32x32x16_bf16(__builtin_bit_cast(bf16x8, a), __builtin_bit_cast(bf16x8, b), c, 0, 0, 0);
}
DI f32x4 mfma16(v4u a, v4u b, f32x4 c) {
  return __builtin_amdgcn_mfma_f32_16x16x32_bf16(__builtin_bit_cast(bf16x8, a), __builtin_bit_cast(bf16x8, b), c, 0, 0, 0);
}
DI u32 pk2(f32x2 v) { return __builtin_bit_cast(u32, __builtin_convertvector(v, bf16x2)); }
DI u32 pk(float a, float b) { return pk2(f32x2{a, b}); }
DI float blo(u32 v) { return __uint_as_float(v << 16); }
DI float bhi(u32 v) { return __uint_as_float(v & 0xffff0000u); }
DI f32x2 unp(u32 v) { return f32x2{blo(v), bhi(v)}; }
DI f32x2 fma2(f32x2 a, f32x2 b, f32x2 c) { return __builtin_elementwise_fma(a, b, c); }
DI f32x2 spl(float a) { return f32x2{a, a}; }
DI float dot2(u32 a, u32 b, float c) { return __builtin_amdgcn_fdot2_f32_bf16(__builtin_bit_cast(bf16x2, a), __builtin_bit_cast(bf16x2, b), c, false); }
template <int C> DI float dpp(float v) { return __int_as_float(__builtin_amdgcn_mov_dpp(__float_as_int(v), C, 0xF, 0xF, true)); }
DI float x16sum(float v) { u32 x = __float_as_uint(v); auto r = __builtin_amdgcn_permlane16_swap(x, x, false, false); return __uint_as_float(r[0]) + __uint_as_float(r[1]); }
DI float x32sum(float v) { u32 x = __float_as_uint(v); auto r = __builtin_amdgcn_permlane32_swap(x, x, false, false); return __uint_as_float(r[0]) + __uint_as_float(r[1]); }
DI float x32max(float v) { u32 x = __float_as_uint(v); auto r = __builtin_amdgcn_permlane32_swap(x, x, false, false); return fmaxf(__uint_as_float(r[0]), __uint_as_float(r[1])); }
DI float x32min(float v) { u32 x = __float_as_uint(v); auto r = __builtin_amdgcn_permlane32_swap(x, x, false, false); return fminf(__uint_as_float(r[0]), __uint_as_float(r[1])); }
DI float hsum32(float v) {
  v += dpp<0xB1>(v); v += dpp<0x4E>(v); v += dpp<0x141>(v); v += dpp<0x140>(v);
  return x16sum(v);
}
template <bool B> struct BoolC { static constexpr bool value = B; };
DI f32x16 zero16() { f32x16 z;
#pragma unroll
  for (int j = 0; j < 16; ++j) z[j] = 0.f;
  return z; }

DI v4u lo8(const f32x16& a) { return v4u{pk(a[0], a[1]), pk(a[2], a[3]), pk(a[4], a[5]), pk(a[6], a[7])}; }
DI v4u hi8(const f32x16& a) { return v4u{pk(a[8], a[9]), pk(a[10], a[11]), pk(a[12], a[13]), pk(a[14], a[15])}; }
DI void publish(__amdgpu_buffer_rsrc_t rsW, u32 off, u32 tlo, u32 thi, u32 lane) {
  if (lane < 8) st64_sc1(v2u{tlo, thi}, rsW, WS_FLAGS + lane * FL_REP + off);
}
typedef u64 v8q __attribute__((ext_vector_type(8)));
DI u64 diff8(const v8q& a, u64 t) { return (((a[0] ^ t) | (a[1] ^ t)) | ((a[2] ^ t) | (a[3] ^ t))) | (((a[4] ^ t) | (a[5] ^ t)) | ((a[6] ^ t) | (a[7] ^ t))); }
DI void spoll16(const char* p, u64 t) {
#pragma unroll 1
  for (u32 spin = 0; spin < SPIN_CAP; ++spin) {
    v8q a, b;
    asm volatile("s_load_dwordx16 %0, %2, 0x0 glc\n\ts_load_dwordx16 %1, %2, 0x40 glc\n\ts_waitcnt lgkmcnt(0)" : "=&s"(a), "=&s"(b) : "s"(p) : "memory");
    if ((diff8(a, t) | diff8(b, t)) == 0) break;
    __builtin_amdgcn_s_sleep(PSLEEP);
  }
}
DI void spoll24(const char* p, const char* q, u64 t) {
#pragma unroll 1
  for (u32 spin = 0; spin < SPIN_CAP; ++spin) {
    v8q a, b, c;
    asm volatile("s_load_dwordx16 %0, %3, 0x0 glc\n\ts_load_dwordx16 %1, %3, 0x40 glc\n\ts_load_dwordx16 %2, %4, 0x0 glc\n\ts_waitcnt lgkmcnt(0)"
                 : "=&s"(a), "=&s"(b), "=&s"(c) : "s"(p), "s"(q) : "memory");
    if ((diff8(a, t) | diff8(b, t) | diff8(c, t)) == 0) break;
    __builtin_amdgcn_s_sleep(PSLEEP);
  }
}
DI void spoll32(const char* p, u64 t) {
#pragma unroll 1
  for (u32 spin = 0; spin < SPIN_CAP; ++spin) {
    v8q a, b, c, d;
    asm volatile("s_load_dwordx16 %0, %4, 0x0 glc\n\ts_load_dwordx16 %1, %4, 0x40 glc\n\ts_load_dwordx16 %2, %4, 0x80 glc\n\ts_load_dwordx16 %3, %4, 0xc0 glc\n\ts_waitcnt lgkmcnt(0)"
                 : "=&s"(a), "=&s"(b), "=&s"(c), "=&s"(d) : "s"(p) : "memory");
    if ((diff8(a, t) | diff8(b, t) | diff8(c, t) | diff8(d, t)) == 0) break;
    __builtin_amdgcn_s_sleep(PSLEEP);
  }
}
DI void vpoll128(v4u rsW, u32 off, u32 tlo, u32 thi) {
#pragma unroll 1
  for (u32 spin = 0; spin < SPIN_CAP; ++spin) {
    v4u t;
    asm volatile("buffer_load_dwordx4 %0, %1, %2, 0 offen sc1\n\ts_waitcnt vmcnt(0)" : "=&v"(t) : "v"(off), "s"(rsW) : "memory");
    if (__all(t.x == tlo && t.y == thi && t.z == tlo && t.w == thi)) break;
    __builtin_amdgcn_s_sleep(PSLEEP);
  }
}
DI u64 tag64(u32 lo, u32 hi) { return ((u64)rfl(hi) << 32) | rfl(lo); }

DI float max3(float a, float b, float c) { float r; asm("v_max3_f32 %0, %1, %2, %3" : "=v"(r) : "v"(a), "v"(b), "v"(c)); return r; }
DI float min3(float a, float b, float c) { float r; asm("v_min3_f32 %0, %1, %2, %3" : "=v"(r) : "v"(a), "v"(b), "v"(c)); return r; }
DI void dual_exp(const f32x16& z, int o, float c, f32x2 (&e)[4], f32x2 (&g)[4]) {
  f32x2 p[4] = {{z[o], z[o + 1]}, {z[o + 2], z[o + 3]}, {z[o + 4], z[o + 5]}, {z[o + 6], z[o + 7]}};
  float mx = __builtin_amdgcn_fmed3f(p[0].x, p[0].y, __builtin_inff()), mn = __builtin_amdgcn_fmed3f(p[0].x, p[0].y, -__builtin_inff());
  mx = max3(max3(max3(mx, p[1].x, p[1].y), p[2].x, p[2].y), p[3].x, p[3].y);
  mn = min3(min3(min3(mn, p[1].x, p[1].y), p[2].x, p[2].y), p[3].x, p[3].y);
  {
    u32 a = __float_as_uint(mx), b = __float_as_uint(mn);
    auto r = __builtin_amdgcn_permlane32_swap(a, a, false, false);
    auto t = __builtin_amdgcn_permlane32_swap(b, b, false, false);
    mx = max3(__uint_as_float(r[0]), __uint_as_float(r[1]), __uint_as_float(r[1]));
    mn = min3(__uint_as_float(t[0]), __uint_as_float(t[1]), __uint_as_float(t[1]));
  }
  const f32x2 a = spl(-mx * c), b = spl(mn * c), c2 = spl(c), n2 = spl(-c);
#pragma unroll
  for (int i = 0; i < 4; ++i) {
    f32x2 t = fma2(p[i], c2, a), u = fma2(p[i], n2, b);
    e[i] = f32x2{__builtin_amdgcn_exp2f(t.x), __builtin_amdgcn_exp2f(t.y)};
    g[i] = f32x2{__builtin_amdgcn_exp2f(u.x), __builtin_amdgcn_exp2f(u.y)};
  }
}
DI void dual_softmax(const f32x16& z, int o, float c, v4u& fp, v4u& fn) {
  f32x2 e[4], g[4];
  dual_exp(z, o, c, e, g);
  f32x2 se = (e[0] + e[1]) + (e[2] + e[3]), sg = (g[0] + g[1]) + (g[2] + g[3]);
  const f32x2 ie = spl(__builtin_amdgcn_rcpf(x32sum(se.x + se.y))), ig = spl(__builtin_amdgcn_rcpf(x32sum(sg.x + sg.y)));
  fp = v4u{pk2(e[0] * ie), pk2(e[1] * ie), pk2(e[2] * ie), pk2(e[3] * ie)};
  fn = v4u{pk2(g[0] * ig), pk2(g[1] * ig), pk2(g[2] * ig), pk2(g[3] * ig)};
}
template <int G> DI void wait_ahead(u32 n) {
  if (n >= 3) asm volatile("s_waitcnt vmcnt(%0)" ::"n"(3 * G) : "memory");
  else if (n == 2) asm volatile("s_waitcnt vmcnt(%0)" ::"n"(2 * G) : "memory");
  else if (n == 1) asm volatile("s_waitcnt vmcnt(%0)" ::"n"(G) : "memory");
  else asm volatile("s_waitcnt vmcnt(0)" ::: "memory");
}
DI bool go_seen(u32 a, u32 magic) { return rfl(*(volatile LAS u32*)(size_t)a) == magic; }
constexpr u32 SYN = 5 * 24576 + 2 * 6144;
constexpr u32 GO0 = 163828, MAGIC0 = 0x7fc3c7e9u, GO1 = 163832, GO2 = 163836, MAGIC1 = 0x7fc1a5e3u, MAGIC2 = 0x7fc2b6d4u, DONE = 0x7fd37fe5u;

constexpr u32 RED = 98304;
DI void sum_selectors(u32 lane, v4u& s0, v4u& s1) {
  const u32 l31 = lane & 31;
  const u32 j = (l31 & 3) + ((l31 >> 3) << 2), sv = (j & 1) ? 0x3f800000u : 0x00003f80u, m = l31 < 16 ? (j >> 1) : 9u;
  const v4u sa = {m == 0 ? sv : 0u, m == 1 ? sv : 0u, m == 2 ? sv : 0u, m == 3 ? sv : 0u};
  const bool up = (l31 >> 2) & 1;
  s0 = v4u{up ? 0u : sa.x, up ? 0u : sa.y, up ? 0u : sa.z, up ? 0u : sa.w};
  s1 = v4u{sa.x ^ s0.x, sa.y ^ s0.y, sa.z ^ s0.z, sa.w ^ s0.w};
  PIN(s0); PIN(s1);
}
DI v4u gelu_frag(u32 a, v4u s0, v4u s1, const f32x2 (&bv)[4]) {
  f32x16 t = mfma(s0, lds128(a), zero16());
  t = mfma(s1, lds128(a + 512), t);
  t = mfma(s0, lds128(a + 24576), t);
  t = mfma(s1, lds128(a + 24576 + 512), t);
  f32x2 v[4] = {f32x2{t[0], t[1]} + bv[0], f32x2{t[2], t[3]} + bv[1], f32x2{t[4], t[5]} + bv[2], f32x2{t[6], t[7]} + bv[3]};
  v4u o;
#pragma unroll
  for (int i = 0; i < 4; ++i) {
    f32x2 z = v[i] * fma2(v[i] * v[i], spl(-2.f * 0.7978845608028654f * 0.044715f * LOG2E), spl(-2.f * 0.7978845608028654f * LOG2E));
    f32x2 d = f32x2{__builtin_amdgcn_exp2f(z.x), __builtin_amdgcn_exp2f(z.y)} + spl(1.f);
    o[i] = pk2(v[i] * f32x2{__builtin_amdgcn_rcpf(d.x), __builtin_amdgcn_rcpf(d.y)});
  }
  return o;
}

#define STAMP(i) do {} while (0)

#define TS(base, n) do {} while (0)
#define TSDUMP(base, sec) do {} while (0)
#define VBART(n, base, k) VBAR(n)
__global__ __launch_bounds__(768, 3) void fused_kernel(const u16* __restrict__ gx, const u16* __restrict__ gwqkv, const u16* __restrict__ gwphi,
                                                         const u16* __restrict__ gwo, const u16* __restrict__ gw1, const u16* __restrict__ gb1,
                                                         const u16* __restrict__ gw2, const u16* __restrict__ gb2, u16* __restrict__ gout, char* ws) {
  STAMP(0);
  const u32 tid = threadIdx.x, lane = tid & 63;
  const u32 w = rfl(tid >> 6);
  const u32 b = blockIdx.x, xcd = b & 7, idx = b >> 3, li = idx >> 1;
  const bool roleA = (idx & 1) == 0;
  const u32 lane16 = lane * 16;
  const u32 tt = (xcd >> 1) * 8 + (idx >> 2), dt = (xcd & 1) * 4 + (idx & 3);
  const u32 frot = (4 * (idx >> 2) + 16 * ((idx >> 1) & 1)) & 31;
  const u32 rt = (xcd >> 1) * 4 + (li >> 2), hg = (xcd & 1) * 4 + (li & 3);
  const u32 bid = xcd * 16 + li;
  const u32 d64 = li & 7, ft = xcd * 2 + (li >> 3);
  const u32 brot = (4 * (li & 7) + 16 * (li >> 3)) & 31;
  const u32 g0 = (ft * 8) / 3;

  if (w >= 8) {
    const u32 lw = w - 8;
    if (lw == 3) {
      const v4u rsT = mkrs(ws, WS_KVR);
      const u32 to = (lane == 0 ? WS_FLAGS : (lane == 1 ? WS_STATS : (lane == 2 ? WS_KVZ : (lane == 3 ? WS_CS : (lane == 4 ? WS_CS + 32768 : WS_KVR))))) + b * 128;
      asm volatile("s_mov_b32 m0, %0\n\ts_nop 0\n\tbuffer_load_dword %1, %2, 0 offen sc1 lds" ::"s"(roleA ? 150528u : 5 * 24576 + 3 * 6144), "v"(to), "s"(rsT) : "memory");
    }
    const bool wo = lw == 3;
    const u32 fpitch = wo ? 1024u : 4096u, fsh = wo ? 14u : 16u;
    const u32 fcol = wo ? dt * 128 : tt * 128, fcnt = wo ? 32u : (lw == 2 ? 28u : 29u);
    const char* fbase = wo ? (const char*)gwo : (const char*)gw2 + (size_t)(lw * 29) * 65536;
    const v4u rsF = mkrs(fbase + fcol - 4096, (fcnt << fsh) - fcol + 4096);
    const u32 fv0 = (lane >> 3) * fpitch + (((lane & 7) ^ (((lane >> 4) & 1) << 2)) << 4) + 4096;
    const u32 fv1 = fv0 + 8 * fpitch - 1024, fv2 = fv0 - 2048, fv3 = fv1 - 2048;
    auto fin = [&](u32 st, u32 slot) {
      u32 i = (2 * st + frot) & 31;
      gl4(slot * 16384 + lw * 4096, fv0, fv1, fv2, fv3, rsF, i << fsh, (i + 1) << fsh);
    };
    if (roleA) {
      const v4u rsX = mkrs((const char*)gx - 4096, 2048 * 1024 + 4096), rsQ = mkrs((const char*)gwqkv - 4096, 512 * 3072 + 4096);
      const u32 rot = (2 * ((li >> 2) + (li & 3))) & 7;
      u32 xv[4], wv[6];
#pragma unroll
      for (int i = 0; i < 4; ++i) {
        u32 row = 32 * lw + 8 * i + (lane >> 3);
        xv[i] = (rt * 128 + row) * 1024 + (((lane & 7) ^ ((row >> 1) & 7)) << 4) + 4096 - i * 1024;
      }
#pragma unroll
      for (int i = 0; i < 3; ++i) {
        u32 o = i * 1024 + lane16, row = o / 384, c = (o - row * 384) >> 4;
        wv[i] = (16 * lw + row) * 3072 + (c >> 3) * 1024 + hg * 128 + (((c & 7) ^ (((row >> 1) & 1) << 2)) << 4) + 4096 - i * 1024;
        wv[3 + i] = wv[i] + 8 * 3072;
      }
      auto stage = [&](u32 st, u32 slot) {
        u32 k = (st + rot) & 7, la = slot * 40960;
        gl4(la + lw * 4096, xv[0], xv[1], xv[2], xv[3], rsX, k * 128, k * 128);
        gl3(la + 16384 + lw * 6144, wv[0], wv[1], wv[2], rsQ, k * (64 * 3072));
        gl3(la + 16384 + lw * 6144 + 3072, wv[3], wv[4], wv[5], rsQ, k * (64 * 3072));
      };
      stage(0, 0); stage(1, 1);
      VBAR(10);
      stage(2, 2);
      u32 slot = 0;
#pragma unroll 1
      for (u32 j = 0; j < 5; ++j) { VBART(10, 139264, j); stage(j + 3, slot); TS(139264, 3 * j + 2); slot = slot == 2 ? 0u : slot + 1; }
      VBART(10, 139264, 5); VBART(0, 139264, 6); LBAR(); TS(139264, 22); TSDUMP(139264, 0);
      fin(0, 0); fin(1, 1);
      LBAR(); LBAR(); LBAR();
      LBAR();
      fin(2, 2); fin(3, 3);
      LBAR(); LBAR();
      LBAR();
    } else {
      const v4u rs1 = mkrs((const char*)gw1 + (size_t)(lw * 512) * 2752 + g0 * 64 - 4096, NOB ? 0u : 512 * 2752 - g0 * 64 + 4096);
      u32 wv[3];
#pragma unroll
      for (int i = 0; i < 3; ++i) { u32 o = i * 1024 + lane16, row = o / 192; wv[i] = row * 2752 + (o - row * 192) + 4096 - i * 1024; }
      auto stage = [&](u32 st, u32 slot) {
        u32 so = ((2 * st + brot) & 31) * (16 * 2752), la = slot * 24576 + lw * 6144;
        gl3(la, wv[0], wv[1], wv[2], rs1, so);
        gl3(la + 3072, wv[0], wv[1], wv[2], rs1, so + 16 * 2752);
      };
      v2u lb0, lb1;
      asm volatile("buffer_load_dwordx2 %0, %2, %3, 0 offen\n\tbuffer_load_dwordx2 %1, %2, %3, 0 offen offset:16"
                   : "=&v"(lb0), "=&v"(lb1) : "v"((g0 * 32 + (2 + lw) * 16 + 4 * (lane >> 5)) * 2), "s"(mkrs(gb1, 1376 * 2)) : "memory");
      u32 is = 0, slot = 0;
      auto next = [&]() { stage(is, slot); ++is; slot = slot == 5 ? 0u : slot + 1; };
      LBAR();
#pragma unroll 1
      for (u32 k = 0; k < PRE0; ++k) { next(); asm volatile("s_waitcnt vmcnt(0)" ::: "memory"); }
#pragma unroll 1
      while (is < 5 && (is == 0 || !go_seen(SYN + 68, 1u))) next();
#pragma unroll 1
      for (u32 spin = 0; spin < (SPIN_CAP << 4) && !go_seen(SYN + 68, 1u); ++spin) __builtin_amdgcn_s_sleep(1);

#pragma unroll 1
      for (u32 j = 0; j < 15; ++j) {
        if (is <= j) next();
        wait_ahead<6>(is - j - 1); TS(155648, 3 * j); LBAR(); TS(155648, 3 * j + 1);
        const u32 lw_ = PACE == 2 ? (j + JIT > 6 ? j + JIT : 6u) : (PACE ? 6 + (j * 3 + 3) / 4 - (j == 0) : j + 6), lim = lw_ < 16 ? lw_ : 16u;
        if (is < lim) next();
        if (is < lim) next();
        TS(155648, 3 * j + 2);
      }
      fin(0, 0); fin(1, 1); fin(2, 2);
      VBART(12, 155648, 15); TSDUMP(155648, 0); LBAR();
      {
        PIN(lb0); PIN(lb1);
        const u32 gown = (ft == 15 ? 43u : ((ft + 1) * 8) / 3) - g0, gs = 2 + lw;
        v4u s0, s1;
        sum_selectors(lane, s0, s1);
        f32x2 bv[4] = {unp(lb0.x), unp(lb0.y), unp(lb1.x), unp(lb1.y)};
        u32 ra = RED + (((lane >> 5) * 2 + 1) * 6 + gs) * 1024 + (lane & 31) * 16, uo = WS_U + ((d64 * 2 + 1) * 86 + g0 * 2 + gs) * 1024 + lane16;
        PIN(bv[0]); PIN(bv[1]); PIN(bv[2]); PIN(bv[3]); PIN(ra); PIN(uo);
        LBAR();
        const v4u o = gelu_frag(ra, s0, s1, bv);
        if ((gs >> 1) < gown) st128_sc1(o, mkrsb(ws, WS_BYTES), uo);
      }
      DRAIN_BAR();
    }
    {
      u32 is = roleA ? 4u : 3u, slot = is;
      auto next = [&]() { fin(is, slot); ++is; slot = slot == 8 ? 0u : slot + 1; };
      if (!roleA) next();
      next(); next();
      VBAR(8);
      LBAR();
#pragma unroll 1
      while (is < 13 && !go_seen(GO2, MAGIC2)) next();
#pragma unroll 1
      for (u32 j = 4; j < 16; ++j) {
        if (is <= j) next();
        wait_ahead<4>(is - j - 1); LBAR();
        const u32 lim = j + FJIT < 16 ? j + FJIT : 16u;
        if (is < lim) next();
      }
    }
    TSDUMP(155648, 1);
    LBAR(); LBAR();
    return;
  }

  const u32 l31 = lane & 31, hh = lane >> 5, blk = (lane >> 4) & 1, q4 = (lane & 15) >> 2, p4 = lane & 3;
  const v4u rsW = mkrs(ws, WS_BYTES);
  const __amdgpu_buffer_rsrc_t rsWb = mkrsb(ws, WS_BYTES);
  const v4u rsX = mkrs(gx, 2048 * 1024);
  const u32 dh = w & 1, kq = w >> 1;
  u32 tlo, thi;

  if (roleA) {
    const u32 th = w & 1, hp = (w >> 1) & 1, fat = w < 4 ? 1u : 0u, tg = 2 * th + 1 - fat;
    v4u wph;
    const u32 XF = 65536 + (w & 3) * 4096 + lane16, IMG = 98304;
    const u32 SIDE = 122880 + (w & 3) * 6144 + lane16;
    const u32 swz = ((l31 >> 1) & 1) | ((((l31 >> 2) ^ (l31 >> 3)) & 1) << 1);
    const u32 wa2 = IMG + (hp * 4 + tg) * 6144 + l31 * 64 + hh * 8 + (swz << 4);
    f32x16 qa0, qa1;
    v4u kf0, kf1;
    float rstd = 0.f;
    tlo = 0; thi = 0;
    if (fat) {
      u32 xa0 = (64 * th + l31) * 128 + ((hh ^ ((l31 >> 1) & 7)) << 4);
      u32 wa = 16384 + (8 * hh + q4) * 384 + ((hp ^ (q4 >> 1)) << 6) + blk * 32 + p4 * 8;
      f32x16 acc[2][3] = {{zero16(), zero16(), zero16()}, {zero16(), zero16(), zero16()}};
      v4u xf[2][2], wf[2][3];
      PIN(acc[0][0]); PIN(acc[0][1]); PIN(acc[0][2]); PIN(acc[1][0]); PIN(acc[1][1]); PIN(acc[1][2]); PIN(xa0); PIN(wa);
      BAR();
      STAMP(1);
      xf[0][0] = lds128(xa0); xf[0][1] = lds128(xa0 + 4096);
#pragma unroll
      for (int s = 0; s < 3; ++s) wf[0][s] = trfrag(wa + s * 128, wa + 1536 + s * 128);
      u32 sb = 0;
#pragma unroll 1
      for (u32 st = 0; st < 8; ++st) {
#pragma unroll
        for (int ks = 0; ks < 4; ++ks) {
          const int c = ks & 1, n = c ^ 1, nk = (ks + 1) & 3;
          if (ks < 3) {
            const u32 xa = sb + (xa0 ^ (nk << 5)), wb = sb + wa + nk * 6144;
            acc[0][0] = mfma(wf[c][0], xf[c][0], acc[0][0]); SCHED();
            xf[n][0] = lds128(xa); wf[n][0] = trfrag(wb, wb + 1536); SCHED();
            acc[1][0] = mfma(wf[c][0], xf[c][1], acc[1][0]); SCHED();
            xf[n][1] = lds128(xa + 4096); SCHED();
            acc[0][1] = mfma(wf[c][1], xf[c][0], acc[0][1]); SCHED();
            wf[n][1] = trfrag(wb + 128, wb + 1536 + 128); SCHED();
            acc[1][1] = mfma(wf[c][1], xf[c][1], acc[1][1]); SCHED();
            wf[n][2] = trfrag(wb + 256, wb + 1536 + 256); SCHED();
            acc[0][2] = mfma(wf[c][2], xf[c][0], acc[0][2]);
            acc[1][2] = mfma(wf[c][2], xf[c][1], acc[1][2]); SCHED();
          } else {
            acc[0][0] = mfma(wf[c][0], xf[c][0], acc[0][0]);
            acc[1][0] = mfma(wf[c][0], xf[c][1], acc[1][0]);
            acc[0][1] = mfma(wf[c][1], xf[c][0], acc[0][1]); SCHED();
            BAR();
            sb = sb == 81920 ? 0u : sb + 40960;
            const u32 xa = sb + xa0, wb = sb + wa;
            xf[n][0] = lds128(xa); wf[n][0] = trfrag(wb, wb + 1536); SCHED();
            acc[1][1] = mfma(wf[c][1], xf[c][1], acc[1][1]); SCHED();
            xf[n][1] = lds128(xa + 4096); wf[n][1] = trfrag(wb + 128, wb + 1536 + 128); SCHED();
            acc[0][2] = mfma(wf[c][2], xf[c][0], acc[0][2]); SCHED();
            wf[n][2] = trfrag(wb + 256, wb + 1536 + 256); SCHED();
            acc[1][2] = mfma(wf[c][2], xf[c][1], acc[1][2]); SCHED();
          }
        }
      }
      STAMP(2);
      v4u ca[3];
#pragma unroll
      for (int s = 0; s < 3; ++s) { v2u c = lds64(SIDE + 3072 + s * 1024); ca[s] = v4u{c.x, c.y, 0u, 0u}; }
      const v4u stg[2] = {lds128(SIDE + 1024), lds128(SIDE + 2048)};
      wph = lds128(SIDE);
      const v4u mb[2] = {{stg[0].x, stg[0].y, 0u, 0u}, {stg[1].x, stg[1].y, 0u, 0u}};
      tlo = rfl(stg[0].w); thi = rfl(stg[1].w);
      acc[1][1] = mfma(ca[1], mb[1], acc[1][1]); acc[0][1] = mfma(ca[1], mb[0], acc[0][1]);
      acc[1][2] = mfma(ca[2], mb[1], acc[1][2]); acc[0][2] = mfma(ca[2], mb[0], acc[0][2]);
      acc[1][0] = mfma(ca[0], mb[1], acc[1][0]); acc[0][0] = mfma(ca[0], mb[0], acc[0][0]);
      sts128(XF, lo8(acc[1][1])); sts128(XF + 1024, hi8(acc[1][1]));
      BAR();
      kf0 = lo8(acc[0][1]); kf1 = hi8(acc[0][1]);
#pragma unroll
      for (int t = 0; t < 2; ++t) {
        const f32x2 r2 = spl(__uint_as_float(stg[t].z));
#pragma unroll
        for (int qq = 0; qq < 4; ++qq)
          sts64((wa2 ^ (qq << 4)) + 4096 + t * 6144, v2u{pk2(f32x2{acc[t][2][4 * qq], acc[t][2][4 * qq + 1]} * r2), pk2(f32x2{acc[t][2][4 * qq + 2], acc[t][2][4 * qq + 3]} * r2)});
      }
      qa0 = acc[0][0]; qa1 = acc[1][0];
      rstd = __uint_as_float(stg[0].z);
    } else {
      v2u ep;
      ld64_sc1_new(ep, 0u, rsW, WS_EPOCH);
      {
        const u16* p = gwphi + (hg * 4 + hp * 2 + (l31 >> 4)) * 256 + (4 * hh) * 16 + (l31 & 15);
        wph.x = p[0] | ((u32)p[16] << 16); wph.y = p[32] | ((u32)p[48] << 16);
        wph.z = p[128] | ((u32)p[144] << 16); wph.w = p[160] | ((u32)p[176] << 16);
      }
      sts128(SIDE, wph);
      asm volatile("s_waitcnt vmcnt(0)" : "+v"(ep)::"memory");
      tlo = rfl(ep.x) + 1u; thi = rfl(ep.y);
      const u32 tB = tlo * 0x9E3779B1u + thi;
      const u32 stoff = WS_STATS + (rt * 128 + 64 * th + l31) * 16, csoff = WS_CS + (hg * 192 + hp * 32 + l31) * 32 + hh * 16;
      BAR();
      LBAR(); LBAR(); LBAR(); LBAR(); LBAR();
      v4u s0, s1, c0, c1, c2;
      asm volatile("buffer_load_dwordx4 %0, %5, %7, 0 offen sc1\n\tbuffer_load_dwordx4 %1, %5, %7, 0 offen offset:512 sc1\n\t"
                   "buffer_load_dwordx4 %2, %6, %7, 0 offen sc1\n\tbuffer_load_dwordx4 %3, %6, %7, 0 offen offset:2048 sc1\n\t"
                   "buffer_load_dwordx4 %4, %6, %7, %8 offen sc1\n\t"
                   "s_barrier\n\ts_barrier\n\ts_waitcnt vmcnt(0)"
                   : "=&v"(s0), "=&v"(s1), "=&v"(c0), "=&v"(c1), "=&v"(c2) : "v"(stoff), "v"(csoff), "s"(rsW), "s"(4096u) : "memory");
#pragma unroll 1
      for (u32 spin = 0; spin < SPIN_CAP; ++spin) {
        const u32 da = (s0.y ^ tlo) | (s1.y ^ tlo) | (c0.y ^ tlo) | (c1.y ^ tlo) | (c2.y ^ tlo);
        const u32 db = (s0.w ^ tB) | (s1.w ^ tB) | (c0.w ^ tB) | (c1.w ^ tB) | (c2.w ^ tB);
        if (__all((da | db) == 0u)) break;
        asm volatile("buffer_load_dwordx4 %0, %5, %7, 0 offen sc1\n\tbuffer_load_dwordx4 %1, %5, %7, 0 offen offset:512 sc1\n\t"
                   "buffer_load_dwordx4 %2, %6, %7, 0 offen sc1\n\tbuffer_load_dwordx4 %3, %6, %7, 0 offen offset:2048 sc1\n\t"
                   "buffer_load_dwordx4 %4, %6, %7, %8 offen sc1\n\t"
                   "s_waitcnt vmcnt(0)"
                   : "=&v"(s0), "=&v"(s1), "=&v"(c0), "=&v"(c1), "=&v"(c2) : "v"(stoff), "v"(csoff), "s"(rsW), "s"(4096u) : "memory");
      }
      {
        const u32 n0 = hh ? 0u : s0.x, n1 = hh ? 0u : s1.x;
        sts128(SIDE + 1024, v4u{(n0 & 0xffffu) | (n0 << 16), n0 >> 16, s0.z, tlo});
        sts128(SIDE + 2048, v4u{(n1 & 0xffffu) | (n1 << 16), n1 >> 16, s1.z, thi});
        rstd = __uint_as_float(s1.z);
        const float cc[3] = {x32sum(__uint_as_float(c0.x) + __uint_as_float(c0.z)), x32sum(__uint_as_float(c1.x) + __uint_as_float(c1.z)),
                             x32sum(__uint_as_float(c2.x) + __uint_as_float(c2.z))};
#pragma unroll
        for (int s = 0; s < 3; ++s) { const u32 hi = pk(cc[s], cc[s]); sts64(SIDE + 3072 + s * 1024, v2u{pk(cc[s], cc[s] - blo(hi)), hi}); }
      }
      BAR();
      BAR();
      kf0 = lds128(XF); kf1 = lds128(XF + 1024);
    }
    const float cexp = rstd * LOG2E;
    const bool lo16 = l31 < 16;
    const v4u wph0 = {lo16 ? wph.x : 0u, lo16 ? wph.y : 0u, lo16 ? wph.z : 0u, lo16 ? wph.w : 0u};
    const v4u wph1 = {wph.x ^ wph0.x, wph.y ^ wph0.y, wph.z ^ wph0.z, wph.w ^ wph0.w};
    {
      f32x16 pkk = mfma(wph1, kf1, mfma(wph0, kf0, zero16()));
#pragma unroll
      for (int hd = 0; hd < 2; ++hd) {
        v4u fp, fn;
        dual_softmax(pkk, 8 * hd, cexp, fp, fn);
        sts64((wa2 ^ 0) + hd * 2048, v2u{fp.x, fp.y});
        sts64((wa2 ^ 16) + hd * 2048, v2u{fp.z, fp.w});
        sts64((wa2 ^ 32) + hd * 2048, v2u{fn.x, fn.y});
        sts64((wa2 ^ 48) + hd * 2048, v2u{fn.z, fn.w});
      }
    }
    if (QS != 3) STAMP(9);
    BAR();
    const v4u ones = {0x3f803f80u, 0x3f803f80u, 0x3f803f80u, 0x3f803f80u};
    {
      const u32 g = lane >> 4, hd = (w >> 1) & 1;
      u32 ra[2];
#pragma unroll
      for (int t = 0; t < 2; ++t) {
        u32 row = 8 * g + 4 * t + q4;
        u32 sw = ((row >> 1) & 1) | ((((row >> 2) ^ (row >> 3)) & 1) << 1);
        ra[t] = IMG + (w >> 2) * 24576 + row * 64 + (((p4 >> 1) ^ sw) << 4) + (p4 & 1) * 8;
      }
      const u32 oa = hd * 2048, xa = (w & 1) << 5, xb = hd << 5;
      f32x4 kv = {0.f, 0.f, 0.f, 0.f}, zz = kv;
#pragma unroll
      for (int t4 = 0; t4 < 4; ++t4) {
        v4u af = trfrag((ra[0] ^ xa) + oa + t4 * 6144, (ra[1] ^ xa) + oa + t4 * 6144);
        v4u vf = trfrag((ra[0] ^ xb) + 4096 + t4 * 6144, (ra[1] ^ xb) + 4096 + t4 * 6144);
        kv = mfma16(af, vf, kv); zz = mfma16(af, ones, zz);
      }
      st64_sc1(v2u{pk(kv[0], kv[1]), pk(kv[2], kv[3])}, rsWb, WS_KVR + ((hg * 8 + w) * 16 + rt) * 512 + lane * 8);
      if ((lane & 15) == 0) st64_sc1(v2u{pk(zz[0], zz[1]), pk(zz[2], zz[3])}, rsWb, WS_KVZ + ((hg * 8 + w) * 16 + rt) * 32 + g * 8);
    }
    if (QS != 3) STAMP(10);
    DRAIN_BAR();
    if (w == 0) publish(rsWb, F2 + (hg * 16 + rt) * 8, tlo, thi, lane);
    STAMP(3);
    v4u pq[2][2];
    {
      v4u qf0, qf1;
      if (fat) {
        sts128(XF + 2048, lo8(qa1)); sts128(XF + 3072, hi8(qa1));
        BAR();
        qf0 = lo8(qa0); qf1 = hi8(qa0);
      } else {
        BAR();
        qf0 = lds128(XF + 2048); qf1 = lds128(XF + 3072);
      }
      f32x16 pqq = mfma(wph1, qf1, mfma(wph0, qf0, zero16()));
#pragma unroll
      for (int hd = 0; hd < 2; ++hd) {
        f32x2 e[4], g[4];
        dual_exp(pqq, 8 * hd, cexp, e, g);
        pq[hd][0] = v4u{pk2(e[0]), pk2(e[1]), pk2(e[2]), pk2(e[3])};
        pq[hd][1] = v4u{pk2(g[0]), pk2(g[1]), pk2(g[2]), pk2(g[3])};
        PIN(pq[hd][0]); PIN(pq[hd][1]);
      }
    }
    const u32 KZ = 151552, ZR = 159744;
    const u32 sv = l31 < 8 ? ((l31 & 1) ? 0x3f800000u : 0x00003f80u) : 0u, sm_ = l31 >> 1;
    v4u sel = {sm_ == 0 ? sv : 0u, sm_ == 1 ? sv : 0u, sm_ == 2 ? sv : 0u, sm_ == 3 ? sv : 0u};
    u32 ka = KZ + hp * 4096 + (lo16 ? (hh * 16 + l31) * 8 : (l31 < 24 ? 512 + hh * 8 : 528u));
    u32 oo = WS_O + ((rt * 4 + tg) * 32 + hg * 4 + hp * 2) * 1024 + lane16;
    u32 za = ZR + w * 512 + (8 * ((lane >> 4) & 1) + q4) * 32 + p4 * 8, zw = KZ + w * 1024 + 512 + (lane >> 5) * 256 + ((lane >> 4) & 1) * 8 + (lane & 1) * 16;
    u32 zs_ = ZR + w * 512 + l31 * 16, kw = KZ + w * 1024 + (2 * l31 + hh) * 8;
    PIN(sel); PIN(ka); PIN(oo); PIN(za); PIN(zw); PIN(zs_); PIN(kw);
    if (w == 0) spoll16(ws + WS_FLAGS + xcd * FL_REP + F2 + hg * 128, tag64(tlo, thi));
    BAR();
    STAMP(4);
    {
      v4u pr[8], pz;
      ld128_sc1_new(pz, l31 * 16, rsW, WS_KVZ + (hg * 8 + w) * 512);
#pragma unroll
      for (int i = 0; i < 8; ++i) ld128_sc1_new(pr[i], lane16, rsW, WS_KVR + (hg * 8 + w) * 8192 + ((i + 2 * (li >> 2)) & 7) * 1024);
      asm volatile("s_waitcnt vmcnt(8)" : "+v"(pz)::"memory");
      FSTAMP(11);
      sts128(zs_, pz);
      const u32 on = hh ? 0u : 0x3f803f80u;
      f32x4 zs = mfma16(trfrag(za, za + 128), v4u{on, on, on, on}, f32x4{0.f, 0.f, 0.f, 0.f});
      sts64(zw,
            (lane & 1) ? v2u{0x3f803f80u, 0x3f803f80u} : v2u{pk(zs[0], zs[1]), pk(zs[2], zs[3])});
      f32x16 sm = zero16();
      asm volatile("s_waitcnt vmcnt(4)" : "+v"(pr[0]), "+v"(pr[1]), "+v"(pr[2]), "+v"(pr[3])::"memory");
#pragma unroll
      for (int i = 0; i < 8; ++i) {
        if (i == 4) asm volatile("s_waitcnt vmcnt(0)" : "+v"(pr[4]), "+v"(pr[5]), "+v"(pr[6]), "+v"(pr[7])::"memory");
        sm = mfma(sel, pr[i], sm);
      }
      sts64(kw, v2u{pk(sm[0], sm[1]), pk(sm[2], sm[3])});
    }
    FSTAMP(9);
    BAR();
    {
#pragma unroll
      for (int hd = 0; hd < 2; ++hd) {
        v2u x0 = lds64(ka + hd * 2048), y0 = lds64(ka + hd * 2048 + 256), x1 = lds64(ka + hd * 2048 + 1024), y1 = lds64(ka + hd * 2048 + 1280);
        f32x16 p = mfma(v4u{x0.x, x0.y, y0.x, y0.y}, pq[hd][0], zero16());
        f32x16 n = mfma(v4u{x1.x, x1.y, y1.x, y1.y}, pq[hd][1], zero16());
        const float inv = __builtin_amdgcn_rcpf(fmaf(p[8], n[12], fmaf(n[8], p[12], 1e-6f * p[12] * n[12])));
        const f32x2 ap = spl(n[12] * inv), an = spl(p[12] * inv);
        v4u o;
#pragma unroll
        for (int j = 0; j < 4; ++j) o[j] = pk2(fma2(f32x2{p[2 * j], p[2 * j + 1]}, ap, f32x2{n[2 * j], n[2 * j + 1]} * an));
        st128_sc1(o, rsWb, oo + hd * 1024);
      }
    }
    FSTAMP(10);
    DRAIN_BAR();
    if (w == 0) publish(rsWb, F3 + (rt * 8 + hg) * 8, tlo, thi, lane);
    STAMP(5);
  } else {
    v4u xr[2];
    v2u ep;
    {
      const u32 xo = hh * 1024 + l31 * 16;
      asm volatile("buffer_load_dwordx4 %0, %2, %3, %4 offen\n\tbuffer_load_dwordx4 %1, %2, %3, %4 offen offset:512"
                   : "=&v"(xr[0]), "=&v"(xr[1]) : "v"(xo), "s"(rsX), "s"((bid * 16 + 2 * w) * 1024) : "memory");
    }
    ld64_sc1_new(ep, 0u, rsW, WS_EPOCH);
    if (w == 0 && lane < 32) *(volatile LAS u32*)(size_t)(SYN + lane * 4) = 0u;
    BAR();
    auto all8 = [&](u32 a) {
#pragma unroll 1
      for (u32 spin = 0; spin < (SPIN_CAP << 4); ++spin)
        if (__all(*(volatile LAS u32*)(size_t)(a + (lane & 7) * 4) == 1u)) break;
    };
    const u32 gown = (ft == 15 ? 43u : ((ft + 1) * 8) / 3) - g0;
    const u32 fd = w >= 6 ? 1u : 0u, gs = w - 6 * fd;
    v2u bias0, bias1;
    {
      const v4u rsB = mkrs(gb1, 1376 * 2);
      const u32 bo = (g0 * 32 + gs * 16 + 4 * hh) * 2;
      asm volatile("buffer_load_dwordx2 %0, %2, %3, 0 offen\n\tbuffer_load_dwordx2 %1, %2, %3, 0 offen offset:16" : "=&v"(bias0), "=&v"(bias1) : "v"(bo), "s"(rsB) : "memory");
    }
    asm volatile("s_waitcnt vmcnt(2)" : "+v"(xr[0]), "+v"(xr[1]), "+v"(ep)::"memory");
    STAMP(1);
    tlo = rfl(ep.x) + 1u; thi = rfl(ep.y);
    const u32 HIM = 147456;
    v4u stat;
    {
      f32x2 v[8];
#pragma unroll
      for (int j = 0; j < 4; ++j) { u32 a = xr[0][j], c = xr[1][j]; v[j] = unp(a); v[4 + j] = unp(c); }
      f32x2 s = ((v[0] + v[1]) + (v[2] + v[3])) + ((v[4] + v[5]) + (v[6] + v[7]));
      const float m = hsum32(s.x + s.y) * (1.f / 512.f);
      f32x2 q = {0.f, 0.f}, q1 = q;
#pragma unroll
      for (int j = 0; j < 8; j += 2) { v[j] -= spl(m); v[j + 1] -= spl(m); q = fma2(v[j], v[j], q); q1 = fma2(v[j + 1], v[j + 1], q1); }
      q += q1;
      const float var = hsum32(q.x + q.y) * (1.f / 512.f);
      const float r1 = __builtin_amdgcn_rsqf(var + 1e-5f);
      const f32x2 rr = spl(r1 * __builtin_amdgcn_rsqf(fmaf(var * r1, r1, 1e-5f)));
      const u32 row = 2 * w + hh, ha = HIM + row * 1024 + ((l31 ^ ((row & 3) << 2)) << 4);
      sts128(ha, v4u{pk2(v[0] * rr), pk2(v[1] * rr), pk2(v[2] * rr), pk2(v[3] * rr)});
      sts128(ha + 512, v4u{pk2(v[4] * rr), pk2(v[5] * rr), pk2(v[6] * rr), pk2(v[7] * rr)});
      const u32 mh = pk(-m, -m);
      stat = v4u{pk(-m, -m - blo(mh)), tlo, __float_as_uint(r1), tlo * 0x9E3779B1u + thi};
    }
    STAMP(9);
    asm volatile("s_waitcnt lgkmcnt(0)" ::: "memory");
    *(volatile LAS u32*)(size_t)(SYN + w * 4) = 1u;
    all8(SYN);
#pragma unroll
    for (int i = 0; i < 2; ++i) {
      const u32 d32 = 2 * w + i;
      const u32 a = HIM + (8 * hh + q4) * 1024 + (((d32 * 4 + 2 * blk + (p4 >> 1)) ^ (q4 << 2)) << 4) + (p4 & 1) * 8;
      st128_sc1(trfrag(a, a + 4096), rsWb, WS_H + (d32 * 128 + bid) * 1024 + lane16);
    }
    STAMP(10);
    asm volatile("s_waitcnt vmcnt(0)" : "+v"(bias0), "+v"(bias1)::"memory");
    *(volatile LAS u32*)(size_t)(SYN + 32 + w * 4) = 1u;
    if (l31 == 0) st128_sc1(stat, rsWb, WS_STATS + (bid * 16 + 2 * w + hh) * 16);
    v4u s0, s1;
    sum_selectors(lane, s0, s1);
    f32x2 bv[4] = {unp(bias0.x), unp(bias0.y), unp(bias1.x), unp(bias1.y)};
    u32 ra = RED + ((hh * 2 + fd) * 6 + gs) * 1024 + l31 * 16, uo = WS_U + ((d64 * 2 + fd) * 86 + g0 * 2 + gs) * 1024 + lane16;
    PIN(bv[0]); PIN(bv[1]); PIN(bv[2]); PIN(bv[3]); PIN(ra); PIN(uo);
    f32x16 acc[3] = {zero16(), zero16(), zero16()};
    u32 wa = kq * 6144 + (8 * hh + q4) * 192 + blk * 32 + p4 * 8;
    PIN(acc[0]); PIN(acc[1]); PIN(acc[2]); PIN(wa);
    if (w == 0) {
      all8(SYN + 32);
      publish(rsWb, F1 + bid * 8, tlo, thi, lane);
      *(volatile LAS u32*)(size_t)(SYN + 64) = 1u;
      STAMP(2);
      __builtin_amdgcn_s_sleep(PDLY);
      vpoll128(rsW, WS_FLAGS + xcd * FL_REP + F1 + lane16, tlo, thi);
      *(volatile LAS u32*)(size_t)(SYN + 68) = 1u;
    } else {
      if (w == 7 && li < 12) {
        all8(SYN + 32);
        const u32 chg = (xcd & 1) * 4 + li / 3, csg = li % 3, cq = xcd >> 1;
        const v4u rsQ = mkrs((const char*)gwqkv + cq * (128 * 3072) + csg * 1024 + chg * 128 - 4096, 128 * 3072 + 4096 - csg * 1024 - chg * 128);
        const u32 v0 = (lane >> 3) * 3072 + (((lane & 7) ^ (((lane >> 4) & 1) << 2)) << 4) + 4096, v1 = v0 + 8 * 3072 - 1024;
#pragma unroll
        for (int i = 0; i < 4; ++i) gl4(HIM + i * 4096, v0, v1, v0 - 2048, v1 - 2048, rsQ, i * (32 * 3072), i * (32 * 3072) + 16 * 3072);
        const u32 ca = HIM + (8 * hh + q4) * 128 + ((q4 >> 1) << 6) + blk * 32 + p4 * 8;
        const v4u ones = {0x3f803f80u, 0x3f803f80u, 0x3f803f80u, 0x3f803f80u};
        f32x16 c0 = zero16(), c1 = zero16();
        asm volatile("s_waitcnt vmcnt(0)" ::: "memory");
#pragma unroll
        for (int ks = 0; ks < 8; ++ks) {
          c0 = mfma(ones, trfrag(ca + ks * 2048, ca + ks * 2048 + 512), c0);
          c1 = mfma(ones, trfrag((ca ^ 64) + ks * 2048, (ca ^ 64) + ks * 2048 + 512), c1);
        }
        st64_sc1(v2u{__float_as_uint(hh ? c1[0] : c0[0]), (cq & 1) ? tlo * 0x9E3779B1u + thi : tlo}, rsWb, WS_CS + ((chg * 3 + csg) * 64 + lane) * 32 + cq * 8);
      }
#pragma unroll 1
      for (u32 spin = 0; spin < (SPIN_CAP << 4) && !go_seen(SYN + 68, 1u); ++spin) __builtin_amdgcn_s_sleep(1);
    }
    STAMP(3);
    const v4u rsH = mkrs(ws + WS_H + ((d64 * 2 + dh) * 128 + kq * 32) * 1024, NOB ? 0u : 32768u);
    v4u hf[8];
#pragma unroll
    for (int i = 0; i < 8; ++i) ld128_sc1_new(hf[i], lane16, rsH, ((brot + i) & 31) * 1024);
    LBAR();
    v4u wf[2][3];
    auto rd = [&](int buf, u32 a) {
#pragma unroll
      for (int g = 0; g < 3; ++g) wf[buf][g] = trfrag(a + g * 64, a + 768 + g * 64);
    };
    rd(0, wa);
    u32 sb = 0, so = brot * 1024;
    auto round = [&](auto last) {
      constexpr bool LAST = decltype(last)::value;
#pragma unroll
      for (int i = 0; i < 8; ++i) {
        const int c = i & 1, n = c ^ 1;
        if (LAST) asm volatile("s_waitcnt vmcnt(%1)" : "+v"(hf[i]) : "n"(7 - i) : "memory");
        else asm volatile("s_waitcnt vmcnt(7)" : "+v"(hf[i])::"memory");
        SCHED();
        acc[0] = mfma(wf[c][0], hf[i], acc[0]); SCHED();
        if (c) { BAR(); sb = sb == 5 * 24576 ? 0u : sb + 24576; }
        const u32 a = sb + wa + (c ? 0 : 3072);
        wf[n][0] = trfrag(a, a + 768); wf[n][1] = trfrag(a + 64, a + 768 + 64); SCHED();
        acc[1] = mfma(wf[c][1], hf[i], acc[1]); SCHED();
        wf[n][2] = trfrag(a + 128, a + 768 + 128); SCHED();
        acc[2] = mfma(wf[c][2], hf[i], acc[2]);
        if (!LAST) ld128_sc1(hf[i], lane16, rsH, (so + i * 1024) & 32767);
        SCHED();
      }
    };
#pragma unroll 1
    for (u32 r = 0; r < 3; ++r) { so += 8192; round(BoolC<false>{});
    }
    round(BoolC<true>{});
    STAMP(4);
#pragma unroll
    for (int g = 0; g < 3; ++g) {
      sts128(RED + (w * 6 + 2 * g) * 1024 + lane16, v4u{pk(acc[g][0], acc[g][1]), pk(acc[g][2], acc[g][3]), pk(acc[g][4], acc[g][5]), pk(acc[g][6], acc[g][7])});
      sts128(RED + (w * 6 + 2 * g + 1) * 1024 + lane16, v4u{pk(acc[g][8], acc[g][9]), pk(acc[g][10], acc[g][11]), pk(acc[g][12], acc[g][13]), pk(acc[g][14], acc[g][15])});
    }
    FSTAMP(11);
    BAR();
    FSTAMP(9);
    {
      const v4u o = gelu_frag(ra, s0, s1, bv);
      if ((gs >> 1) < gown) st128_sc1(o, rsWb, uo);
    }
    FSTAMP(10);
    DRAIN_BAR();
    if (w == 0) publish(rsWb, F4 + (d64 * 16 + ft) * 8, tlo, thi, lane);
    STAMP(5);
  }

  const bool isWO = kq == 3;
  v4u xres;
  u32 b2r, eoff;
  v4u S0, S1, S2, S3;
  auto body = [&](auto wo_) {
    constexpr bool WO = decltype(wo_)::value;
    f32x16 acc[2] = {zero16(), zero16()};
    u32 fa = kq * 4096 + (4 * hh + q4) * 128 + ((q4 >> 1) << 6) + blk * 32 + p4 * 8;
    const v4u rsA = WO ? mkrs(ws + WS_O + (tt * 2 + dh) * 32768, 32768) : mkrs(ws + WS_U + ((dt * 2 + dh) * 86 + kq * 29) * 1024, kq == 2 ? 28 * 1024 : 29 * 1024);
    PIN(acc[0]); PIN(acc[1]); PIN(fa);
    asm volatile("" ::"s"(rsA));
    LBAR();
    v4u rf[8][2];
#pragma unroll
    for (int i = 0; i < 8; ++i) {
      const u32 a = fa + (i >> 1) * 16384 + (i & 1) * 2048;
      rf[i][0] = trfrag(a, a + 1024);
      rf[i][1] = trfrag(a ^ 64, (a ^ 64) + 1024);
    }
    asm volatile("s_waitcnt lgkmcnt(0)\n\ts_barrier"
                 : "+v"(rf[0][0]), "+v"(rf[0][1]), "+v"(rf[1][0]), "+v"(rf[1][1]), "+v"(rf[2][0]), "+v"(rf[2][1]), "+v"(rf[3][0]), "+v"(rf[3][1]),
                   "+v"(rf[4][0]), "+v"(rf[4][1]), "+v"(rf[5][0]), "+v"(rf[5][1]), "+v"(rf[6][0]), "+v"(rf[6][1]), "+v"(rf[7][0]), "+v"(rf[7][1])::"memory");
    if (!WO && w == 0) {
      spoll24(ws + WS_FLAGS + xcd * FL_REP + F4 + dt * 128, ws + WS_FLAGS + xcd * FL_REP + F3 + (tt >> 1) * 64, tag64(tlo, thi));
      *(volatile LAS u32*)(size_t)GO2 = MAGIC2;
      if (b == 0 && lane == 0) st64_sc1(v2u{tlo, thi}, rsWb, WS_EPOCH);
    }
    LBAR();
    STAMP(6);
    auto mm = [&](v4u a, v4u wgt, f32x16& c) { c = WO ? mfma(wgt, a, c) : mfma(a, wgt, c); };
    v4u af[8];
#pragma unroll
    for (int i = 0; i < 8; ++i) ld128_sc1_new(af[i], lane16, rsA, ((frot + i) & 31) * 1024);
    v4u wf[2][2];
    auto rd = [&](int buf, u32 a) {
      wf[buf][0] = trfrag(a, a + 1024);
      wf[buf][1] = trfrag(a ^ 64, (a ^ 64) + 1024);
    };
    u32 sb = 4 * 16384, so = frot * 1024 + 8192;
#pragma unroll
    for (int i = 0; i < 8; ++i) {
      asm volatile("s_waitcnt vmcnt(7)" : "+v"(af[i])::"memory");
      SCHED();
      mm(af[i], rf[i][0], acc[0]); mm(af[i], rf[i][1], acc[1]);
      ld128_sc1(af[i], lane16, rsA, (so + i * 1024) & 32767);
      if (i == 6) rd(0, sb + fa);
      SCHED();
    }
    auto round = [&](auto last) {
      constexpr bool LAST = decltype(last)::value;
#pragma unroll
      for (int i = 0; i < 8; ++i) {
        const int c = i & 1, n = c ^ 1;
        if (LAST) asm volatile("s_waitcnt vmcnt(%1)" : "+v"(af[i]) : "n"(9 - i) : "memory");
        else asm volatile("s_waitcnt vmcnt(7)" : "+v"(af[i])::"memory");
        SCHED();
        mm(af[i], wf[c][0], acc[0]); SCHED();
        if (c) { BAR(); sb = sb == 8 * 16384 ? 0u : sb + 16384; }
        rd(n, sb + fa + (c ? 0 : 2048)); SCHED();
        mm(af[i], wf[c][1], acc[1]);
        if (!LAST) ld128_sc1(af[i], lane16, rsA, (so + i * 1024) & 32767);
        SCHED();
      }
    };
#pragma unroll 1
    for (u32 r = 0; r < 2; ++r) { so += 8192; round(BoolC<false>{}); }
    {
      u32 ln = lane;
      PIN(ln);
      const u32 tk = tt * 64 + ((w >> 1) & 1) * 32 + (ln & 31);
      eoff = (tk * 512 + dt * 64 + (w >> 2) * 32 + (w & 1) * 16 + (ln >> 5) * 8) * 2;
      ld128_new(xres, eoff, rsX, 0u);
      asm volatile("buffer_load_ushort %0, %1, %2, 0 offen" : "=v"(b2r) : "v"(tk * 2), "s"(mkrs(gb2, 4096)) : "memory");
    }
    {
      u32 ln = lane;
      PIN(ln);
      const u32 sv = (ln & 1) ? 0x3f800000u : 0x00003f80u, m = (ln >> 1) & 3, g = (ln >> 3) & 3, h2 = (ln >> 5) * 2;
      const v4u e = {m == 0 ? sv : 0u, m == 1 ? sv : 0u, m == 2 ? sv : 0u, m == 3 ? sv : 0u}, z = {0u, 0u, 0u, 0u};
      S0 = g == 0 ? e : z; S1 = g == 1 ? e : z; S2 = g == h2 ? e : z; S3 = g == h2 + 1 ? e : z;
      PIN(S0); PIN(S1); PIN(S2); PIN(S3);
    }
    round(BoolC<true>{});
    STAMP(7);
    {
      const u32 pb = kq * 8192 + dh * (WO ? 2048u : 4096u) + lane16, pi = WO ? 4096u : 2048u;
      sts128(pb, lo8(acc[0])); sts128(pb + 1024, hi8(acc[0]));
      sts128(pb + pi, lo8(acc[1])); sts128(pb + pi + 1024, hi8(acc[1]));
    }
    asm volatile("s_waitcnt vmcnt(0)" : "+v"(xres), "+v"(b2r)::"memory");
  };
  if (isWO) body(BoolC<true>{}); else body(BoolC<false>{});
  BAR();
  {
    const u32 a = (lane16 >> 9) * 8192 + w * 1024 + (lane16 & 511);
    f32x16 t = mfma(S0, lds128(a), zero16());
    t = mfma(S1, lds128(a + 512), t);
    t = mfma(S2, lds128(a + 16384), t);
    t = mfma(S3, lds128(a + 16384 + 512), t);
    const float b2v = blo(b2r);
    v4u o;
#pragma unroll
    for (int j = 0; j < 4; ++j) {
      f32x2 yy = f32x2{t[2 * j], t[2 * j + 1]} + spl(b2v), e = yy * spl(-LOG2E);
      f32x2 d = f32x2{__builtin_amdgcn_exp2f(e.x), __builtin_amdgcn_exp2f(e.y)} + spl(1.f);
      f32x2 g = yy * f32x2{__builtin_amdgcn_rcpf(d.x), __builtin_amdgcn_rcpf(d.y)};
      u32 xd = xres[j];
      o[j] = pk2(fma2(f32x2{t[8 + 2 * j], t[9 + 2 * j]}, g, unp(xd)));
    }
    *(v4u*)((char*)gout + eoff) = o;
  }
}

extern "C" void kernel_launch(void* const* d_in, const int* in_sizes, int n_in, void* d_out, int out_size, void* d_ws, size_t ws_size,
                              hipStream_t stream) {
  (void)in_sizes; (void)n_in; (void)out_size; (void)ws_size;
  (void)hipFuncSetAttribute((const void*)fused_kernel, hipFuncAttributeMaxDynamicSharedMemorySize, LDS_BYTES);
  fused_kernel<<<256, 768, LDS_BYTES, stream>>>((const u16*)d_in[0], (const u16*)d_in[1], (const u16*)d_in[2], (const u16*)d_in[3],
                                                (const u16*)d_in[5], (const u16*)d_in[6], (const u16*)d_in[7], (const u16*)d_in[8],
                                                (u16*)d_out, (char*)d_ws);
}
```

```cpp
#include <hip/hip_runtime.h>
#include <stdint.h>

typedef unsigned u32;
typedef unsigned short u16;
typedef unsigned long long u64;
typedef u32 v4u __attribute__((ext_vector_type(4)));
typedef u32 v2u __attribute__((ext_vector_type(2)));
typedef float f32x16 __attribute__((ext_vector_type(16)));
typedef float f32x4 __attribute__((ext_vector_type(4)));
typedef float f32x2 __attribute__((ext_vector_type(2)));
typedef short s16x4 __attribute__((ext_vector_type(4)));
typedef __bf16 bf16x8 __attribute__((ext_vector_type(8)));
typedef __bf16 bf16x2 __attribute__((ext_vector_type(2)));
#define LAS __attribute__((address_space(3)))
#define DI __device__ __forceinline__

constexpr u32 WS_EPOCH = 0, WS_FLAGS = 4096, FL_REP = 16384;
constexpr u32 F1 = 0, F2 = 8192, F3 = 9216, F4 = 10240;
constexpr u32 WS_STATS = 139264;
constexpr u32 WS_KVZ = 180224;
constexpr u32 WS_CS = 212992;
constexpr u32 WS_KVR = 262144;
constexpr u32 WS_H = 1u << 20;
constexpr u32 WS_O = 3u << 20;
constexpr u32 WS_U = 5u << 20;
constexpr u32 WS_BYTES = 7u << 20;
constexpr u32 LDS_BYTES = 163840;
constexpr u32 CODE_BYTES = 24 * 1024;
constexpr u32 SPIN_CAP = 1u << 15;
constexpr float LOG2E = 1.4426950408889634f;

DI u32 rfl(u32 v) { return (u32)__builtin_amdgcn_readfirstlane((int)v); }
DI v4u mkrs(const void* p, u32 bytes) { u64 a = (u64)p; return v4u{(u32)a, (u32)(a >> 32) & 0xffffu, bytes, 0x00020000u}; }
DI __amdgpu_buffer_rsrc_t mkrsb(const void* p, u32 bytes) { return __builtin_amdgcn_make_buffer_rsrc((void*)p, (short)0, (int)bytes, 0x00020000); }
DI void gl4(u32 m, u32 v0, u32 v1, u32 v2, u32 v3, v4u rs, u32 sa, u32 sb) {
  asm volatile("s_mov_b32 m0, %0\n\ts_nop 0\n\t"
               "buffer_load_dwordx4 %1, %5, %6 offen lds\n\tbuffer_load_dwordx4 %2, %5, %6 offen offset:1024 lds\n\t"
               "buffer_load_dwordx4 %3, %5, %7 offen offset:2048 lds\n\tbuffer_load_dwordx4 %4, %5, %7 offen offset:3072 lds"
               ::"s"(m), "v"(v0), "v"(v1), "v"(v2), "v"(v3), "s"(rs), "s"(sa), "s"(sb) : "memory");
}
DI void gl3(u32 m, u32 v0, u32 v1, u32 v2, v4u rs, u32 sa) {
  asm volatile("s_mov_b32 m0, %0\n\ts_nop 0\n\t"
               "buffer_load_dwordx4 %1, %4, %5 offen lds\n\tbuffer_load_dwordx4 %2, %4, %5 offen offset:1024 lds\n\t"
               "buffer_load_dwordx4 %3, %4, %5 offen offset:2048 lds"
               ::"s"(m), "v"(v0), "v"(v1), "v"(v2), "s"(rs), "s"(sa) : "memory");
}
DI void ld128_sc1(v4u& d, u32 voff, v4u rs, u32 soff) {
  asm volatile("buffer_load_dwordx4 %0, %1, %2, %3 offen sc1" : "+v"(d) : "v"(voff), "s"(rs), "s"(soff) : "memory");
}
DI void ld128_sc1_new(v4u& d, u32 voff, v4u rs, u32 soff) {
  asm volatile("buffer_load_dwordx4 %0, %1, %2, %3 offen sc1" : "=v"(d) : "v"(voff), "s"(rs), "s"(soff) : "memory");
}
DI void ld128_new(v4u& d, u32 voff, v4u rs, u32 soff) {
  asm volatile("buffer_load_dwordx4 %0, %1, %2, %3 offen" : "=v"(d) : "v"(voff), "s"(rs), "s"(soff) : "memory");
}
DI void ld64_sc1_new(v2u& d, u32 voff, v4u rs, u32 soff) {
  asm volatile("buffer_load_dwordx2 %0, %1, %2, %3 offen sc1" : "=v"(d) : "v"(voff), "s"(rs), "s"(soff) : "memory");
}
DI void st128_sc1(v4u v, __amdgpu_buffer_rsrc_t rs, u32 off) { __builtin_amdgcn_raw_buffer_store_b128(v, rs, (int)off, 0, 16); }
DI void st64_sc1(v2u v, __amdgpu_buffer_rsrc_t rs, u32 off) { __builtin_amdgcn_raw_buffer_store_b64(v, rs, (int)off, 0, 16); }
#define BAR() asm volatile("s_waitcnt lgkmcnt(0)\n\ts_barrier" ::: "memory")
#define DRAIN_BAR() asm volatile("s_waitcnt vmcnt(0) lgkmcnt(0)\n\ts_barrier" ::: "memory")
#define VBAR(n) asm volatile("s_waitcnt vmcnt(" #n ")\n\ts_barrier" ::: "memory")
#define LBAR() asm volatile("s_barrier" ::: "memory")
#define PIN(x) asm volatile("" : "+v"(x))
#define SCHED() __builtin_amdgcn_sched_barrier(0)

DI v2u trrd(u32 a) { return __builtin_bit_cast(v2u, __builtin_amdgcn_ds_read_tr16_b64_v4i16((LAS s16x4*)(size_t)a)); }
DI v4u trfrag(u32 a0, u32 a1) { v2u x = trrd(a0), y = trrd(a1); return v4u{x.x, x.y, y.x, y.y}; }
DI v4u lds128(u32 a) { return *(LAS v4u*)(size_t)a; }
DI v2u lds64(u32 a) { return *(LAS v2u*)(size_t)a; }
DI f32x4 lds128f(u32 a) { return *(LAS f32x4*)(size_t)a; }
DI void sts128(u32 a, v4u v) { *(LAS v4u*)(size_t)a = v; }
DI void sts64(u32 a, v2u v) { *(LAS v2u*)(size_t)a = v; }
DI void sts32f(u32 a, float v) { *(LAS float*)(size_t)a = v; }
DI f32x16 mfma(v4u a, v4u b, f32x16 c) {
  return __builtin_amdgcn_mfma_f32_32x32x16_bf16(__builtin_bit_cast(bf16x8, a), __builtin_bit_cast(bf16x8, b), c, 0, 0, 0);
}
DI f32x4 mfma16(v4u a, v4u b, f32x4 c) {
  return __builtin_amdgcn_mfma_f32_16x16x32_bf16(__builtin_bit_cast(bf16x8, a), __builtin_bit_cast(bf16x8, b), c, 0, 0, 0);
}
DI u32 pk2(f32x2 v) { return __builtin_bit_cast(u32, __builtin_convertvector(v, bf16x2)); }
DI u32 pk(float a, float b) { return pk2(f32x2{a, b}); }
DI float blo(u32 v) { return __uint_as_float(v << 16); }
DI float bhi(u32 v) { return __uint_as_float(v & 0xffff0000u); }
DI float rb(float v) { return blo(pk(v, v)); }
DI f32x2 unp(u32 v) { return f32x2{blo(v), bhi(v)}; }
DI f32x2 fma2(f32x2 a, f32x2 b, f32x2 c) { return __builtin_elementwise_fma(a, b, c); }
DI f32x2 spl(float a) { return f32x2{a, a}; }
template <int C> DI float dpp(float v) { return __int_as_float(__builtin_amdgcn_mov_dpp(__float_as_int(v), C, 0xF, 0xF, true)); }
DI float x16sum(float v) { u32 x = __float_as_uint(v); auto r = __builtin_amdgcn_permlane16_swap(x, x, false, false); return __uint_as_float(r[0]) + __uint_as_float(r[1]); }
DI float x32sum(float v) { u32 x = __float_as_uint(v); auto r = __builtin_amdgcn_permlane32_swap(x, x, false, false); return __uint_as_float(r[0]) + __uint_as_float(r[1]); }
DI float hsum32(float v) {
  v += dpp<0xB1>(v); v += dpp<0x4E>(v); v += dpp<0x141>(v); v += dpp<0x140>(v);
  return x16sum(v);
}
template <bool B> struct BoolC { static constexpr bool value = B; };
DI f32x16 zero16() { f32x16 z;
#pragma unroll
  for (int j = 0; j < 16; ++j) z[j] = 0.f;
  return z; }

DI v4u lo8(const f32x16& a) { return v4u{pk(a[0], a[1]), pk(a[2], a[3]), pk(a[4], a[5]), pk(a[6], a[7])}; }
DI v4u hi8(const f32x16& a) { return v4u{pk(a[8], a[9]), pk(a[10], a[11]), pk(a[12], a[13]), pk(a[14], a[15])}; }
DI void publish(__amdgpu_buffer_rsrc_t rsW, u32 off, u32 tlo, u32 thi, u32 lane) {
  if (lane < 8) st64_sc1(v2u{tlo, thi}, rsW, WS_FLAGS + lane * FL_REP + off);
}
typedef u64 v8q __attribute__((ext_vector_type(8)));
DI u64 diff8(const v8q& a, u64 t) { return (((a[0] ^ t) | (a[1] ^ t)) | ((a[2] ^ t) | (a[3] ^ t))) | (((a[4] ^ t) | (a[5] ^ t)) | ((a[6] ^ t) | (a[7] ^ t))); }
DI void spoll16(const char* p, u64 t) {
#pragma unroll 1
  for (u32 spin = 0; spin < SPIN_CAP; ++spin) {
    v8q a, b;
    asm volatile("s_load_dwordx16 %0, %2, 0x0 glc\n\ts_load_dwordx16 %1, %2, 0x40 glc\n\ts_waitcnt lgkmcnt(0)" : "=&s"(a), "=&s"(b) : "s"(p) : "memory");
    if ((diff8(a, t) | diff8(b, t)) == 0) break;
    __builtin_amdgcn_s_sleep(2);
  }
}
DI void spoll24(const char* p, const char* q, u64 t) {
#pragma unroll 1
  for (u32 spin = 0; spin < SPIN_CAP; ++spin) {
    v8q a, b, c;
    asm volatile("s_load_dwordx16 %0, %3, 0x0 glc\n\ts_load_dwordx16 %1, %3, 0x40 glc\n\ts_load_dwordx16 %2, %4, 0x0 glc\n\ts_waitcnt lgkmcnt(0)"
                 : "=&s"(a), "=&s"(b), "=&s"(c) : "s"(p), "s"(q) : "memory");
    if ((diff8(a, t) | diff8(b, t) | diff8(c, t)) == 0) break;
    __builtin_amdgcn_s_sleep(2);
  }
}
DI void spoll32(const char* p, u64 t) {
#pragma unroll 1
  for (u32 spin = 0; spin < SPIN_CAP; ++spin) {
    v8q a, b, c, d;
    asm volatile("s_load_dwordx16 %0, %4, 0x0 glc\n\ts_load_dwordx16 %1, %4, 0x40 glc\n\ts_load_dwordx16 %2, %4, 0x80 glc\n\ts_load_dwordx16 %3, %4, 0xc0 glc\n\ts_waitcnt lgkmcnt(0)"
                 : "=&s"(a), "=&s"(b), "=&s"(c), "=&s"(d) : "s"(p) : "memory");
    if ((diff8(a, t) | diff8(b, t) | diff8(c, t) | diff8(d, t)) == 0) break;
    __builtin_amdgcn_s_sleep(2);
  }
}
DI u64 tag64(u32 lo, u32 hi) { return ((u64)rfl(hi) << 32) | rfl(lo); }

DI float max3(float a, float b, float c) { float r; asm("v_max3_f32 %0, %1, %2, %3" : "=v"(r) : "v"(a), "v"(b), "v"(c)); return r; }
DI float min3(float a, float b, float c) { float r; asm("v_min3_f32 %0, %1, %2, %3" : "=v"(r) : "v"(a), "v"(b), "v"(c)); return r; }
DI void dual_exp(const f32x16& z, int o, float c, f32x2 (&e)[4], f32x2 (&g)[4]) {
  f32x2 p[4] = {{z[o], z[o + 1]}, {z[o + 2], z[o + 3]}, {z[o + 4], z[o + 5]}, {z[o + 6], z[o + 7]}};
  float mx = __builtin_amdgcn_fmed3f(p[0].x, p[0].y, __builtin_inff()), mn = __builtin_amdgcn_fmed3f(p[0].x, p[0].y, -__builtin_inff());
  mx = max3(max3(max3(mx, p[1].x, p[1].y), p[2].x, p[2].y), p[3].x, p[3].y);
  mn = min3(min3(min3(mn, p[1].x, p[1].y), p[2].x, p[2].y), p[3].x, p[3].y);
  {
    u32 a = __float_as_uint(mx), b = __float_as_uint(mn);
    auto r = __builtin_amdgcn_permlane32_swap(a, a, false, false);
    auto t = __builtin_amdgcn_permlane32_swap(b, b, false, false);
    mx = max3(__uint_as_float(r[0]), __uint_as_float(r[1]), __uint_as_float(r[1]));
    mn = min3(__uint_as_float(t[0]), __uint_as_float(t[1]), __uint_as_float(t[1]));
  }
  const f32x2 a = spl(-mx * c), b = spl(mn * c), c2 = spl(c), n2 = spl(-c);
#pragma unroll
  for (int i = 0; i < 4; ++i) {
    f32x2 t = fma2(p[i], c2, a), u = fma2(p[i], n2, b);
    e[i] = f32x2{__builtin_amdgcn_exp2f(t.x), __builtin_amdgcn_exp2f(t.y)};
    g[i] = f32x2{__builtin_amdgcn_exp2f(u.x), __builtin_amdgcn_exp2f(u.y)};
  }
}
DI void dual_softmax(const f32x16& z, int o, float c, v4u& fp, v4u& fn) {
  f32x2 e[4], g[4];
  dual_exp(z, o, c, e, g);
  f32x2 se = (e[0] + e[1]) + (e[2] + e[3]), sg = (g[0] + g[1]) + (g[2] + g[3]);
  const f32x2 ie = spl(__builtin_amdgcn_rcpf(x32sum(se.x + se.y))), ig = spl(__builtin_amdgcn_rcpf(x32sum(sg.x + sg.y)));
  fp = v4u{pk2(e[0] * ie), pk2(e[1] * ie), pk2(e[2] * ie), pk2(e[3] * ie)};
  fn = v4u{pk2(g[0] * ig), pk2(g[1] * ig), pk2(g[2] * ig), pk2(g[3] * ig)};
}
template <int G> DI void wait_ahead(u32 n) {
  if (n >= 3) asm volatile("s_waitcnt vmcnt(%0)" ::"n"(3 * G) : "memory");
  else if (n == 2) asm volatile("s_waitcnt vmcnt(%0)" ::"n"(2 * G) : "memory");
  else if (n == 1) asm volatile("s_waitcnt vmcnt(%0)" ::"n"(G) : "memory");
  else asm volatile("s_waitcnt vmcnt(0)" ::: "memory");
}
DI bool go_seen(u32 a, u32 magic) { return rfl(*(volatile LAS u32*)(size_t)a) == magic; }
constexpr u32 SYN = 5 * 24576 + 2 * 6144;
constexpr u32 GO2 = 163836, MAGIC2 = 0x7fc2b6d4u;

constexpr u32 RED = 98304;
DI void sum_selectors(u32 lane, v4u& s0, v4u& s1) {
  const u32 l31 = lane & 31;
  const u32 j = (l31 & 3) + ((l31 >> 3) << 2), sv = (j & 1) ? 0x3f800000u : 0x00003f80u, m = l31 < 16 ? (j >> 1) : 9u;
  const v4u sa = {m == 0 ? sv : 0u, m == 1 ? sv : 0u, m == 2 ? sv : 0u, m == 3 ? sv : 0u};
  const bool up = (l31 >> 2) & 1;
  s0 = v4u{up ? 0u : sa.x, up ? 0u : sa.y, up ? 0u : sa.z, up ? 0u : sa.w};
  s1 = v4u{sa.x ^ s0.x, sa.y ^ s0.y, sa.z ^ s0.z, sa.w ^ s0.w};
  PIN(s0); PIN(s1);
}
DI v4u gelu_frag(u32 a, v4u s0, v4u s1, const f32x2 (&bv)[4]) {
  f32x16 t = mfma(s0, lds128(a), zero16());
  t = mfma(s1, lds128(a + 512), t);
  t = mfma(s0, lds128(a + 24576), t);
  t = mfma(s1, lds128(a + 24576 + 512), t);
  f32x2 v[4] = {f32x2{t[0], t[1]} + bv[0], f32x2{t[2], t[3]} + bv[1], f32x2{t[4], t[5]} + bv[2], f32x2{t[6], t[7]} + bv[3]};
  v4u o;
#pragma unroll
  for (int i = 0; i < 4; ++i) {
    f32x2 z = v[i] * fma2(v[i] * v[i], spl(-2.f * 0.7978845608028654f * 0.044715f * LOG2E), spl(-2.f * 0.7978845608028654f * LOG2E));
    f32x2 d = f32x2{__builtin_amdgcn_exp2f(z.x), __builtin_amdgcn_exp2f(z.y)} + spl(1.f);
    o[i] = pk2(v[i] * f32x2{__builtin_amdgcn_rcpf(d.x), __builtin_amdgcn_rcpf(d.y)});
  }
  return o;
}

DI u32 opq(u32 v) { asm volatile("" : "+s"(v)); return v; }
#define GEO_BASE(b) const u32 xcd = (b) & 7, idx = (b) >> 3, li = idx >> 1; (void)li
#define GEO_A() const u32 rt = (xcd >> 1) * 4 + (li >> 2), hg = (xcd & 1) * 4 + (li & 3)
#define GEO_B() const u32 d64 = li & 7, ft = xcd * 2 + (li >> 3), brot = (4 * (li & 7) + 16 * (li >> 3)) & 31, g0 = (ft * 8) / 3
#define GEO_F() const u32 tt = (xcd >> 1) * 8 + (idx >> 2), dt = (xcd & 1) * 4 + (idx & 3), frot = (4 * (idx >> 2) + 16 * ((idx >> 1) & 1)) & 31
__global__ __launch_bounds__(768, 3) void fused_kernel(const u16* __restrict__ gx, const u16* __restrict__ gwqkv, const u16* __restrict__ gwphi,
                                                         const u16* __restrict__ gwo, const u16* __restrict__ gw1, const u16* __restrict__ gb1,
                                                         const u16* __restrict__ gw2, const u16* __restrict__ gb2, u16* __restrict__ gout, char* ws) {
  const u32 tid = threadIdx.x, lane = tid & 63;
  const u32 w = rfl(tid >> 6);
  const bool roleA = (blockIdx.x & 8) == 0;
  const u32 lane16 = lane * 16;
  u64 pc0;
  asm volatile("s_getpc_b64 %0" : "=s"(pc0));
  auto touch = [&](u32 la) {
    asm volatile("s_mov_b32 m0, %0\n\ts_nop 0\n\tbuffer_load_dword %1, %2, 0 offen lds" ::"s"(la), "v"(lane < 16 ? ((blockIdx.x >> 4) * 16 + lane) * 128 : 0x10000000u),
                            "s"(mkrs((const void*)(pc0 & ~(u64)127), CODE_BYTES)) : "memory");
  };
  {
    const u32 b = blockIdx.x;
    GEO_BASE(b); GEO_A();
    const u32 rot = (2 * ((li >> 2) + (li & 3))) & 7;
    if (roleA) {
      if (w == 4) touch(146432u);
      __builtin_amdgcn_s_sleep(6);
    }
    if (w < 8) {
      const u32 r8 = lane >> 3;
      const u32 v0 = roleA ? r8 * 1024 + (((lane & 7) ^ (r8 >> 1)) << 4) : lane16, v1 = roleA ? (v0 ^ 64) + 7168 : v0;
      const u32 so = roleA ? (rt * 128 + 16 * w) * 1024 + rot * 128 : ((xcd * 16 + li) * 16 + 2 * w) * 1024;
      asm volatile("s_mov_b32 m0, %0\n\ts_nop 0\n\tbuffer_load_dwordx4 %1, %3, %4 offen lds\n\tbuffer_load_dwordx4 %2, %3, %4 offen offset:1024 lds"
                   ::"s"((roleA ? 0u : 147456u) + w * 2048), "v"(v0), "v"(v1), "s"(mkrs(gx, 2048 * 1024)), "s"(so) : "memory");
    }
    if (roleA) {
      u32 v[2];
#pragma unroll
      for (int i = 0; i < 2; ++i) {
        const u32 o = (2 * w + i) * 1024 + lane16, row = (o * 43691u) >> 24, c = (o - row * 384) >> 4;
        v[i] = row * 3072 + (c >> 3) * 1024 + (((c & 7) ^ (((row >> 1) & 1) << 2)) << 4) + 1024 - i * 1024;
      }
      asm volatile("s_mov_b32 m0, %0\n\ts_nop 0\n\tbuffer_load_dwordx4 %1, %3, %4 offen lds\n\tbuffer_load_dwordx4 %2, %3, %4 offen offset:1024 lds"
                   ::"s"(16384 + w * 2048), "v"(v[0]), "v"(v[1]), "s"(mkrs((const char*)gwqkv - 1024, 512 * 3072 + 1024)), "s"(rot * (64 * 3072) + hg * 128) : "memory");
    }
  }

  if (w >= 8) {
    const u32 lw = w - 8;
    v4u rsF;
    u32 fv0, fv1, fv2, fv3, fsh, frot_;
    auto fin_setup = [&]() {
      const u32 b = opq(blockIdx.x);
      GEO_BASE(b); GEO_F();
      const bool wo = lw == 3;
      const u32 fpitch = wo ? 1024u : 4096u, fcol = wo ? dt * 128 : tt * 128, fcnt = wo ? 32u : (lw == 2 ? 28u : 29u);
      const char* fbase = wo ? (const char*)gwo : (const char*)gw2 + (size_t)(lw * 29) * 65536;
      fsh = wo ? 14u : 16u; frot_ = frot;
      rsF = mkrs(fbase + fcol - 4096, (fcnt << fsh) - fcol + 4096);
      fv0 = (lane >> 3) * fpitch + (((lane & 7) ^ (((lane >> 4) & 1) << 2)) << 4) + 4096;
      fv1 = fv0 + 8 * fpitch - 1024; fv2 = fv0 - 2048; fv3 = fv1 - 2048;
      if (lw == 3) {
        const u32 to = lane < 4 ? WS_FLAGS + (b * 4 + lane) * 128 : (lane == 4 ? WS_STATS : (lane == 5 ? WS_KVZ : (lane == 6 ? WS_CS : (lane == 7 ? WS_CS + 16384 : WS_KVR)))) + b * 128;
        asm volatile("s_mov_b32 m0, %0\n\ts_nop 0\n\tbuffer_load_dword %1, %2, 0 offen sc1 lds" ::"s"(roleA ? 150528u : 5 * 24576 + 3 * 6144), "v"(to), "s"(mkrs(ws, WS_KVR)) : "memory");
      }
    };
    auto fin = [&](u32 st, u32 slot) {
      u32 i = (2 * st + frot_) & 31;
      gl4(rfl(slot * 16384 + lw * 4096), fv0, fv1, fv2, fv3, rsF, rfl(i << fsh), rfl((i + 1) << fsh));
    };
    if (roleA) {
      const u32 b = opq(blockIdx.x);
      GEO_BASE(b); GEO_A();
      const v4u rsX = mkrs((const char*)gx - 4096, 2048 * 1024 + 4096);
      const u32 rot = (2 * ((li >> 2) + (li & 3))) & 7;
      u32 xv[4], wv[6];
#pragma unroll
      for (int i = 0; i < 4; ++i) {
        u32 row = 32 * lw + 8 * i + (lane >> 3);
        xv[i] = (rt * 128 + row) * 1024 + (((lane & 7) ^ ((row >> 1) & 7)) << 4) + 4096 - i * 1024;
      }
      const v4u rsQ = mkrs((const char*)gwqkv - 4096, 512 * 3072 + 4096);
#pragma unroll
      for (int i = 0; i < 3; ++i) {
        u32 o = i * 1024 + lane16, row = o / 384, c = (o - row * 384) >> 4;
        wv[i] = (16 * lw + row) * 3072 + (c >> 3) * 1024 + hg * 128 + (((c & 7) ^ (((row >> 1) & 1) << 2)) << 4) + 4096 - i * 1024;
        wv[3 + i] = wv[i] + 8 * 3072;
      }
      auto stage = [&](u32 st, u32 slot) {
        u32 k = (st + rot) & 7, la = slot * 40960;
        gl4(la + lw * 4096, xv[0], xv[1], xv[2], xv[3], rsX, k * 128, k * 128);
        gl3(la + 16384 + lw * 6144, wv[0], wv[1], wv[2], rsQ, k * (64 * 3072));
        gl3(la + 16384 + lw * 6144 + 3072, wv[3], wv[4], wv[5], rsQ, k * (64 * 3072));
      };
      stage(1, 1);
      VBAR(10);
      stage(2, 2);
      fin_setup();
      u32 slot = 0;
#pragma unroll 1
      for (u32 j = 0; j < 5; ++j) { VBAR(10); stage(j + 3, slot); slot = slot == 2 ? 0u : slot + 1; }
      VBAR(10);
      if (lw < 3) {
        const u32 bl = (3u - ((u32)((u64)gw2 >> 7) & 7u)) & 7u, st = (4 * bl + 8 + (li & 7) + 16 * (li >> 3)) & 31;
        const v4u rsB = mkrs((const char*)gw2 + (size_t)(lw * 29) * 65536 + ((xcd >> 1) * 8 + bl) * 128 - 4096, ((lw == 2 ? 28u : 29u) << 16) - 3968 + 4096);
        __builtin_amdgcn_s_sleep(3);
        asm volatile("s_mov_b32 m0, %0\n\ts_nop 0\n\tbuffer_load_dwordx4 %1, %3, %4 offen lds\n\tbuffer_load_dwordx4 %2, %3, %4 offen offset:1024 lds"
                     ::"s"(81920 + lw * 2048), "v"(fv0), "v"(fv1), "s"(rsB), "s"(st << 16) : "memory");
        VBAR(2);
      } else VBAR(0);
      if (lw == 0) touch(150528u);
      LBAR();
      fin(0, 0); fin(1, 1);
      LBAR(); LBAR(); LBAR();
      LBAR();
      if (lw == 0) touch(150528u);
      fin(2, 2); fin(3, 3);
      LBAR(); LBAR();
      VBAR(0);
    } else {
      const u32 b = opq(blockIdx.x);
      GEO_BASE(b); GEO_B();
      const v4u rs1 = mkrs((const char*)gw1 + (size_t)(lw * 512) * 2752 + g0 * 64 - 4096, 512 * 2752 - g0 * 64 + 4096);
      u32 wv[3];
#pragma unroll
      for (int i = 0; i < 3; ++i) { u32 o = i * 1024 + lane16, row = o / 192; wv[i] = row * 2752 + (o - row * 192) + 4096 - i * 1024; }
      auto stage = [&](u32 st, u32 slot) {
        u32 so = ((2 * st + brot) & 31) * (16 * 2752), la = slot * 24576 + lw * 6144;
        gl3(la, wv[0], wv[1], wv[2], rs1, so);
        gl3(la + 3072, wv[0], wv[1], wv[2], rs1, so + 16 * 2752);
      };
      u32 is = 0, slot = 0;
      auto next = [&]() { stage(is, slot); ++is; slot = slot == 5 ? 0u : slot + 1; };
      LBAR();
      next();
      v2u lb0, lb1;
      asm volatile("buffer_load_dwordx2 %0, %2, %3, 0 offen\n\tbuffer_load_dwordx2 %1, %2, %3, 0 offen offset:16"
                   : "=&v"(lb0), "=&v"(lb1) : "v"((g0 * 32 + (2 + lw) * 16 + 4 * (lane >> 5)) * 2), "s"(mkrs(gb1, 1376 * 2)) : "memory");
      fin_setup();
      asm volatile("s_waitcnt vmcnt(0)" : "+v"(lb0), "+v"(lb1)::"memory");
      {
#pragma unroll 1
        for (u32 spin = 0; spin < (SPIN_CAP << 4); ++spin) {
          if (__all(*(volatile LAS u32*)(size_t)(SYN + 32 + (lane & 7) * 4) == 1u)) break;
          __builtin_amdgcn_s_sleep(1);
        }
        next();
      }
#pragma unroll 1
      while (is < 5 && (is == 0 || !go_seen(SYN + 68, 1u))) next();
#pragma unroll 1
      for (u32 spin = 0; spin < (SPIN_CAP << 4) && !go_seen(SYN + 68, 1u); ++spin) __builtin_amdgcn_s_sleep(1);

#pragma unroll 1
      for (u32 j = 0; j < 15; ++j) {
        if (is <= j) next();
        wait_ahead<6>(is - j - 1); LBAR();
        const u32 pace = 6 + (j * 3 + 3) / 4 - (j == 0), lim = pace < 16 ? pace : 16u;
        if (is < lim) next();
        if (is < lim) next();
      }
      fin(0, 0); fin(1, 1); fin(2, 2);
      VBAR(12); LBAR();
      {
        PIN(lb0); PIN(lb1);
        const u32 gown = (ft == 15 ? 43u : ((ft + 1) * 8) / 3) - g0, gs = 2 + lw;
        v4u s0, s1;
        sum_selectors(lane, s0, s1);
        f32x2 bv[4] = {unp(lb0.x), unp(lb0.y), unp(lb1.x), unp(lb1.y)};
        u32 ra = RED + (((lane >> 5) * 2 + 1) * 6 + gs) * 1024 + (lane & 31) * 16, uo = WS_U + ((d64 * 2 + 1) * 86 + g0 * 2 + gs) * 1024 + lane16;
        PIN(bv[0]); PIN(bv[1]); PIN(bv[2]); PIN(bv[3]); PIN(ra); PIN(uo);
        LBAR();
        const v4u o = gelu_frag(ra, s0, s1, bv);
        if ((gs >> 1) < gown) st128_sc1(o, mkrsb(ws, WS_BYTES), uo);
      }
      DRAIN_BAR();
    }
    {
      u32 is = roleA ? 4u : 3u, slot = is;
      auto next = [&]() { fin(is, slot); ++is; slot = slot == 8 ? 0u : slot + 1; };
      if (!roleA) next();
      next(); next();
      VBAR(8);
      LBAR();
#pragma unroll 1
      while (is < 13 && !go_seen(GO2, MAGIC2)) next();
#pragma unroll 1
      for (u32 j = 4; j < 16; ++j) {
        if (is <= j) next();
        wait_ahead<4>(is - j - 1); LBAR();
        const u32 lim = j + 6 < 16 ? j + 6 : 16u;
        if (is < lim) next();
      }
    }
    LBAR(); LBAR();
    return;
  }

  const u32 l31 = lane & 31, hh = lane >> 5, blk = (lane >> 4) & 1, q4 = (lane & 15) >> 2, p4 = lane & 3;
  const v4u rsW = mkrs(ws, WS_BYTES);
  const __amdgpu_buffer_rsrc_t rsWb = mkrsb(ws, WS_BYTES);
  const v4u rsX = mkrs(gx, 2048 * 1024);
  const u32 dh = w & 1, kq = w >> 1;
  u32 tlo, thi;

  if (roleA) {
    const u32 b = opq(blockIdx.x);
    GEO_BASE(b); GEO_A();
    const u32 th = w & 1, hp = (w >> 1) & 1, fat = w < 4 ? 1u : 0u, tg = 2 * th + 1 - fat;
    v4u wph;
    const u32 XF = 65536 + (w & 3) * 4096 + lane16, IMG = 98304;
    const u32 SIDE = 122880 + (w & 3) * 6144 + lane16;
    const u32 swz = ((l31 >> 1) & 1) | ((((l31 >> 2) ^ (l31 >> 3)) & 1) << 1);
    const u32 wa2 = IMG + (hp * 4 + tg) * 6144 + l31 * 64 + hh * 8 + (swz << 4);
    f32x16 qa0, qa1;
    v4u kf0, kf1;
    float rstd = 0.f;
    tlo = 0; thi = 0;
    if (fat) {
      u32 xa0 = (64 * th + l31) * 128 + ((hh ^ ((l31 >> 1) & 7)) << 4);
      u32 wa = 16384 + (8 * hh + q4) * 384 + ((hp ^ (q4 >> 1)) << 6) + blk * 32 + p4 * 8;
      f32x16 acc[2][3] = {{zero16(), zero16(), zero16()}, {zero16(), zero16(), zero16()}};
      v4u xf[2][2], wf[2][3];
      PIN(acc[0][0]); PIN(acc[0][1]); PIN(acc[0][2]); PIN(acc[1][0]); PIN(acc[1][1]); PIN(acc[1][2]); PIN(xa0); PIN(wa);
      DRAIN_BAR();
      xf[0][0] = lds128(xa0); xf[0][1] = lds128(xa0 + 4096);
#pragma unroll
      for (int s = 0; s < 3; ++s) wf[0][s] = trfrag(wa + s * 128, wa + 1536 + s * 128);
      u32 sb = 0;
#pragma unroll 1
      for (u32 st = 0; st < 8; ++st) {
#pragma unroll
        for (int ks = 0; ks < 4; ++ks) {
          const int c = ks & 1, n = c ^ 1, nk = (ks + 1) & 3;
          if (ks < 3) {
            const u32 xa = sb + (xa0 ^ (nk << 5)), wb = sb + wa + nk * 6144;
            acc[0][0] = mfma(wf[c][0], xf[c][0], acc[0][0]); SCHED();
            xf[n][0] = lds128(xa); wf[n][0] = trfrag(wb, wb + 1536); SCHED();
            acc[1][0] = mfma(wf[c][0], xf[c][1], acc[1][0]); SCHED();
            xf[n][1] = lds128(xa + 4096); SCHED();
            acc[0][1] = mfma(wf[c][1], xf[c][0], acc[0][1]); SCHED();
            wf[n][1] = trfrag(wb + 128, wb + 1536 + 128); SCHED();
            acc[1][1] = mfma(wf[c][1], xf[c][1], acc[1][1]); SCHED();
            wf[n][2] = trfrag(wb + 256, wb + 1536 + 256); SCHED();
            acc[0][2] = mfma(wf[c][2], xf[c][0], acc[0][2]);
            acc[1][2] = mfma(wf[c][2], xf[c][1], acc[1][2]); SCHED();
          } else {
            acc[0][0] = mfma(wf[c][0], xf[c][0], acc[0][0]);
            acc[1][0] = mfma(wf[c][0], xf[c][1], acc[1][0]);
            acc[0][1] = mfma(wf[c][1], xf[c][0], acc[0][1]); SCHED();
            BAR();
            sb = sb == 81920 ? 0u : sb + 40960;
            const u32 xa = sb + xa0, wb = sb + wa;
            xf[n][0] = lds128(xa); wf[n][0] = trfrag(wb, wb + 1536); SCHED();
            acc[1][1] = mfma(wf[c][1], xf[c][1], acc[1][1]); SCHED();
            xf[n][1] = lds128(xa + 4096); wf[n][1] = trfrag(wb + 128, wb + 1536 + 128); SCHED();
            acc[0][2] = mfma(wf[c][2], xf[c][0], acc[0][2]); SCHED();
            wf[n][2] = trfrag(wb + 256, wb + 1536 + 256); SCHED();
            acc[1][2] = mfma(wf[c][2], xf[c][1], acc[1][2]); SCHED();
          }
        }
      }
      v4u ca[3];
#pragma unroll
      for (int s = 0; s < 3; ++s) { v2u c = lds64(SIDE + 3072 + s * 1024); ca[s] = v4u{c.x, c.y, 0u, 0u}; }
      const v4u stg[2] = {lds128(SIDE + 1024), lds128(SIDE + 2048)};
      wph = lds128(SIDE);
      const v4u mb[2] = {{stg[0].x, stg[0].y, 0u, 0u}, {stg[1].x, stg[1].y, 0u, 0u}};
      tlo = rfl(stg[0].w); thi = rfl(stg[1].w);
      acc[1][1] = mfma(ca[1], mb[1], acc[1][1]); acc[0][1] = mfma(ca[1], mb[0], acc[0][1]);
      acc[1][2] = mfma(ca[2], mb[1], acc[1][2]); acc[0][2] = mfma(ca[2], mb[0], acc[0][2]);
      acc[1][0] = mfma(ca[0], mb[1], acc[1][0]); acc[0][0] = mfma(ca[0], mb[0], acc[0][0]);
      sts128(XF, lo8(acc[1][1])); sts128(XF + 1024, hi8(acc[1][1]));
      BAR();
      kf0 = lo8(acc[0][1]); kf1 = hi8(acc[0][1]);
#pragma unroll
      for (int t = 0; t < 2; ++t) {
        const f32x2 r2 = spl(__uint_as_float(stg[t].z));
#pragma unroll
        for (int qq = 0; qq < 4; ++qq)
          sts64((wa2 ^ (qq << 4)) + 4096 + t * 6144, v2u{pk2(f32x2{acc[t][2][4 * qq], acc[t][2][4 * qq + 1]} * r2), pk2(f32x2{acc[t][2][4 * qq + 2], acc[t][2][4 * qq + 3]} * r2)});
      }
      qa0 = acc[0][0]; qa1 = acc[1][0];
      rstd = __uint_as_float(stg[0].z);
    } else {
      v2u ep;
      ld64_sc1_new(ep, 0u, rsW, WS_EPOCH);
      {
        const u16* p = gwphi + (hg * 4 + hp * 2 + (l31 >> 4)) * 256 + (4 * hh) * 16 + (l31 & 15);
        wph.x = p[0] | ((u32)p[16] << 16); wph.y = p[32] | ((u32)p[48] << 16);
        wph.z = p[128] | ((u32)p[144] << 16); wph.w = p[160] | ((u32)p[176] << 16);
      }
      sts128(SIDE, wph);
      asm volatile("s_waitcnt vmcnt(0)" : "+v"(ep)::"memory");
      tlo = rfl(ep.x) + 1u; thi = rfl(ep.y);
      const u32 stoff = WS_STATS + (rt * 128 + 64 * th + l31) * 16, csoff = WS_CS + (hg * 192 + hp * 32 + l31) * 32 + hh * 16;
      BAR();
      if ((((li >> 2) + (li & 3)) & 3) == (xcd >> 1)) {
        const u32 wa = 16384 + (16 * (w - 4) + 8 * hh + q4) * 384 + ((q4 >> 1) << 6) + blk * 32 + p4 * 8, CSP = 147456;
        const v4u ones = {0x3f803f80u, 0x3f803f80u, 0x3f803f80u, 0x3f803f80u};
        f32x16 cs[6];
#pragma unroll
        for (int i = 0; i < 6; ++i) { const u32 a = (wa ^ ((i / 3) << 6)) + (i % 3) * 128; cs[i] = mfma(ones, trfrag(a, a + 1536), zero16()); }
        BAR();
#pragma unroll
        for (int i = 0; i < 6; ++i) {
          const u32 a = 40960 + (wa ^ ((i / 3) << 6)) + (i % 3) * 128;
          cs[i] = mfma(ones, trfrag(a, a + 1536), cs[i]);
          sts32f(CSP + i * 512 + l31 * 16 + (w - 4) * 4, cs[i][0]);
        }
        BAR();
        if (w < 7) {
          const f32x4 c4 = lds128f(CSP + (hh * 3 + (w - 4)) * 512 + l31 * 16);
          st64_sc1(v2u{__float_as_uint((c4[0] + c4[1]) + (c4[2] + c4[3])), tlo}, rsWb,
                   WS_CS + ((hg * 3 + (w - 4)) * 64 + lane) * 32 + (xcd >> 1) * 8);
        }
      } else {
        LBAR(); LBAR();
      }
      LBAR(); LBAR(); LBAR(); LBAR();
      v4u s0, s1, c0, c1, c2;
      asm volatile("buffer_load_dwordx4 %0, %5, %7, 0 offen sc1\n\tbuffer_load_dwordx4 %1, %5, %7, 0 offen offset:512 sc1\n\t"
                   "buffer_load_dwordx4 %2, %6, %7, 0 offen sc1\n\tbuffer_load_dwordx4 %3, %6, %7, 0 offen offset:2048 sc1\n\t"
                   "buffer_load_dwordx4 %4, %6, %7, %8 offen sc1\n\t"
                   "s_barrier\n\ts_waitcnt vmcnt(0)"
                   : "=&v"(s0), "=&v"(s1), "=&v"(c0), "=&v"(c1), "=&v"(c2) : "v"(stoff), "v"(csoff), "s"(rsW), "s"(4096u) : "memory");
#pragma unroll 1
      for (u32 spin = 0; spin < SPIN_CAP; ++spin) {
        const u32 da = (s0.y ^ tlo) | (s1.y ^ tlo) | (c0.y ^ tlo) | (c1.y ^ tlo) | (c2.y ^ tlo);
        const u32 db = (s0.w ^ tlo) | (s1.w ^ tlo) | (c0.w ^ tlo) | (c1.w ^ tlo) | (c2.w ^ tlo);
        if (__all((da | db) == 0u)) break;
        asm volatile("buffer_load_dwordx4 %0, %5, %7, 0 offen sc1\n\tbuffer_load_dwordx4 %1, %5, %7, 0 offen offset:512 sc1\n\t"
                   "buffer_load_dwordx4 %2, %6, %7, 0 offen sc1\n\tbuffer_load_dwordx4 %3, %6, %7, 0 offen offset:2048 sc1\n\t"
                   "buffer_load_dwordx4 %4, %6, %7, %8 offen sc1\n\t"
                   "s_waitcnt vmcnt(0)"
                   : "=&v"(s0), "=&v"(s1), "=&v"(c0), "=&v"(c1), "=&v"(c2) : "v"(stoff), "v"(csoff), "s"(rsW), "s"(4096u) : "memory");
      }
      {
        const u32 n0 = hh ? 0u : s0.x, n1 = hh ? 0u : s1.x;
        sts128(SIDE + 1024, v4u{(n0 & 0xffffu) | (n0 << 16), n0 >> 16, s0.z, tlo});
        sts128(SIDE + 2048, v4u{(n1 & 0xffffu) | (n1 << 16), n1 >> 16, s1.z, thi});
        rstd = __uint_as_float(s1.z);
        const float cc[3] = {x32sum(__uint_as_float(c0.x) + __uint_as_float(c0.z)), x32sum(__uint_as_float(c1.x) + __uint_as_float(c1.z)),
                             x32sum(__uint_as_float(c2.x) + __uint_as_float(c2.z))};
#pragma unroll
        for (int s = 0; s < 3; ++s) { const u32 hi = pk(cc[s], cc[s]); sts64(SIDE + 3072 + s * 1024, v2u{pk(cc[s], cc[s] - blo(hi)), hi}); }
      }
      BAR();
      BAR();
      kf0 = lds128(XF); kf1 = lds128(XF + 1024);
    }
    const float cexp = rstd * LOG2E;
    const bool lo16 = l31 < 16;
    const v4u wph0 = {lo16 ? wph.x : 0u, lo16 ? wph.y : 0u, lo16 ? wph.z : 0u, lo16 ? wph.w : 0u};
    const v4u wph1 = {wph.x ^ wph0.x, wph.y ^ wph0.y, wph.z ^ wph0.z, wph.w ^ wph0.w};
    {
      f32x16 pkk = mfma(wph1, kf1, mfma(wph0, kf0, zero16()));
#pragma unroll
      for (int hd = 0; hd < 2; ++hd) {
        v4u fp, fn;
        dual_softmax(pkk, 8 * hd, cexp, fp, fn);
        sts64((wa2 ^ 0) + hd * 2048, v2u{fp.x, fp.y});
        sts64((wa2 ^ 16) + hd * 2048, v2u{fp.z, fp.w});
        sts64((wa2 ^ 32) + hd * 2048, v2u{fn.x, fn.y});
        sts64((wa2 ^ 48) + hd * 2048, v2u{fn.z, fn.w});
      }
    }
    BAR();
    const v4u ones = {0x3f803f80u, 0x3f803f80u, 0x3f803f80u, 0x3f803f80u};
    {
      const u32 g = lane >> 4, hd = (w >> 1) & 1;
      u32 ra[2];
#pragma unroll
      for (int t = 0; t < 2; ++t) {
        u32 row = 8 * g + 4 * t + q4;
        u32 sw = ((row >> 1) & 1) | ((((row >> 2) ^ (row >> 3)) & 1) << 1);
        ra[t] = IMG + (w >> 2) * 24576 + row * 64 + (((p4 >> 1) ^ sw) << 4) + (p4 & 1) * 8;
      }
      const u32 oa = hd * 2048, xa = (w & 1) << 5, xb = hd << 5;
      f32x4 kv = {0.f, 0.f, 0.f, 0.f}, zz = kv;
#pragma unroll
      for (int t4 = 0; t4 < 4; ++t4) {
        v4u af = trfrag((ra[0] ^ xa) + oa + t4 * 6144, (ra[1] ^ xa) + oa + t4 * 6144);
        v4u vf = trfrag((ra[0] ^ xb) + 4096 + t4 * 6144, (ra[1] ^ xb) + 4096 + t4 * 6144);
        kv = mfma16(af, vf, kv); zz = mfma16(af, ones, zz);
      }
      st64_sc1(v2u{pk(kv[0], kv[1]), pk(kv[2], kv[3])}, rsWb, WS_KVR + ((hg * 8 + w) * 16 + rt) * 512 + lane * 8);
      if ((lane & 15) == 0) st64_sc1(v2u{pk(zz[0], zz[1]), pk(zz[2], zz[3])}, rsWb, WS_KVZ + ((hg * 8 + w) * 16 + rt) * 32 + g * 8);
    }
    DRAIN_BAR();
    if (w == 0) publish(rsWb, F2 + (hg * 16 + rt) * 8, tlo, thi, lane);
    v4u pq[2][2];
    {
      v4u qf0, qf1;
      if (fat) {
        sts128(XF + 2048, lo8(qa1)); sts128(XF + 3072, hi8(qa1));
        BAR();
        qf0 = lo8(qa0); qf1 = hi8(qa0);
      } else {
        BAR();
        qf0 = lds128(XF + 2048); qf1 = lds128(XF + 3072);
      }
      f32x16 pqq = mfma(wph1, qf1, mfma(wph0, qf0, zero16()));
#pragma unroll
      for (int hd = 0; hd < 2; ++hd) {
        f32x2 e[4], g[4];
        dual_exp(pqq, 8 * hd, cexp, e, g);
        pq[hd][0] = v4u{pk2(e[0]), pk2(e[1]), pk2(e[2]), pk2(e[3])};
        pq[hd][1] = v4u{pk2(g[0]), pk2(g[1]), pk2(g[2]), pk2(g[3])};
        PIN(pq[hd][0]); PIN(pq[hd][1]);
      }
    }
    const u32 KZ = 151552, ZR = 159744;
    const u32 sv = l31 < 8 ? ((l31 & 1) ? 0x3f800000u : 0x00003f80u) : 0u, sm_ = l31 >> 1;
    v4u sel = {sm_ == 0 ? sv : 0u, sm_ == 1 ? sv : 0u, sm_ == 2 ? sv : 0u, sm_ == 3 ? sv : 0u};
    u32 ka = KZ + hp * 4096 + (lo16 ? (hh * 16 + l31) * 8 : (l31 < 24 ? 512 + hh * 8 : 528u));
    u32 oo = WS_O + ((rt * 4 + tg) * 32 + hg * 4 + hp * 2) * 1024 + lane16;
    u32 za = ZR + w * 512 + (8 * ((lane >> 4) & 1) + q4) * 32 + p4 * 8, zw = KZ + w * 1024 + 512 + (lane >> 5) * 256 + ((lane >> 4) & 1) * 8 + (lane & 1) * 16;
    u32 zs_ = ZR + w * 512 + l31 * 16, kw = KZ + w * 1024 + (2 * l31 + hh) * 8;
    PIN(sel); PIN(ka); PIN(oo); PIN(za); PIN(zw); PIN(zs_); PIN(kw);
    if (w == 0) spoll16(ws + WS_FLAGS + xcd * FL_REP + F2 + hg * 128, tag64(tlo, thi));
    BAR();
    {
      v4u pr[8], pz;
      ld128_sc1_new(pz, l31 * 16, rsW, WS_KVZ + (hg * 8 + w) * 512);
#pragma unroll
      for (int i = 0; i < 8; ++i) ld128_sc1_new(pr[i], lane16, rsW, WS_KVR + (hg * 8 + w) * 8192 + ((i + 2 * (li >> 2)) & 7) * 1024);
      asm volatile("s_waitcnt vmcnt(8)" : "+v"(pz)::"memory");
      sts128(zs_, pz);
      const u32 on = hh ? 0u : 0x3f803f80u;
      f32x4 zs = mfma16(trfrag(za, za + 128), v4u{on, on, on, on}, f32x4{0.f, 0.f, 0.f, 0.f});
      sts64(zw,
            (lane & 1) ? v2u{0x3f803f80u, 0x3f803f80u} : v2u{pk(zs[0], zs[1]), pk(zs[2], zs[3])});
      f32x16 sm = zero16();
      asm volatile("s_waitcnt vmcnt(4)" : "+v"(pr[0]), "+v"(pr[1]), "+v"(pr[2]), "+v"(pr[3])::"memory");
#pragma unroll
      for (int i = 0; i < 8; ++i) {
        if (i == 4) asm volatile("s_waitcnt vmcnt(0)" : "+v"(pr[4]), "+v"(pr[5]), "+v"(pr[6]), "+v"(pr[7])::"memory");
        sm = mfma(sel, pr[i], sm);
      }
      sts64(kw, v2u{pk(sm[0], sm[1]), pk(sm[2], sm[3])});
    }
    BAR();
    {
#pragma unroll
      for (int hd = 0; hd < 2; ++hd) {
        v2u x0 = lds64(ka + hd * 2048), y0 = lds64(ka + hd * 2048 + 256), x1 = lds64(ka + hd * 2048 + 1024), y1 = lds64(ka + hd * 2048 + 1280);
        f32x16 p = mfma(v4u{x0.x, x0.y, y0.x, y0.y}, pq[hd][0], zero16());
        f32x16 n = mfma(v4u{x1.x, x1.y, y1.x, y1.y}, pq[hd][1], zero16());
        const float inv = __builtin_amdgcn_rcpf(fmaf(p[8], n[12], fmaf(n[8], p[12], 1e-6f * p[12] * n[12])));
        const f32x2 ap = spl(n[12] * inv), an = spl(p[12] * inv);
        v4u o;
#pragma unroll
        for (int j = 0; j < 4; ++j) o[j] = pk2(fma2(f32x2{p[2 * j], p[2 * j + 1]}, ap, f32x2{n[2 * j], n[2 * j + 1]} * an));
        st128_sc1(o, rsWb, oo + hd * 1024);
      }
    }
    DRAIN_BAR();
    if (w == 0) publish(rsWb, F3 + (rt * 8 + hg) * 8, tlo, thi, lane);
  } else {
    v2u ep;
    const u32 bid = (blockIdx.x & 7) * 16 + (blockIdx.x >> 4);
    ld64_sc1_new(ep, 0u, rsW, WS_EPOCH);
    if (w == 0 && lane < 32) *(volatile LAS u32*)(size_t)(SYN + lane * 4) = 0u;
    BAR();
    const u32 b = opq(blockIdx.x);
    GEO_BASE(b); GEO_B();
    auto all8 = [&](u32 a) {
#pragma unroll 1
      for (u32 spin = 0; spin < (SPIN_CAP << 4); ++spin)
        if (__all(*(volatile LAS u32*)(size_t)(a + (lane & 7) * 4) == 1u)) break;
      asm volatile("" ::: "memory");
    };
    const u32 gown = (ft == 15 ? 43u : ((ft + 1) * 8) / 3) - g0;
    const u32 fd = w >= 6 ? 1u : 0u, gs = w - 6 * fd;
    v2u bias0, bias1;
    {
      const v4u rsB = mkrs(gb1, 1376 * 2);
      const u32 bo = (g0 * 32 + gs * 16 + 4 * hh) * 2;
      asm volatile("buffer_load_dwordx2 %0, %2, %3, 0 offen\n\tbuffer_load_dwordx2 %1, %2, %3, 0 offen offset:16" : "=&v"(bias0), "=&v"(bias1) : "v"(bo), "s"(rsB) : "memory");
    }
    asm volatile("s_waitcnt vmcnt(3)" ::: "memory");
    const v4u xr[2] = {lds128(147456 + (2 * w + hh) * 1024 + l31 * 16), lds128(147456 + (2 * w + hh) * 1024 + l31 * 16 + 512)};
    const u32 HIM = 147456;
    v4u stat;
    {
      f32x2 v[8];
#pragma unroll
      for (int j = 0; j < 4; ++j) { u32 a = xr[0][j], c = xr[1][j]; v[j] = unp(a); v[4 + j] = unp(c); }
      f32x2 s = ((v[0] + v[1]) + (v[2] + v[3])) + ((v[4] + v[5]) + (v[6] + v[7]));
      const float m = hsum32(s.x + s.y) * (1.f / 512.f);
      f32x2 q = {0.f, 0.f}, q1 = q;
#pragma unroll
      for (int j = 0; j < 8; j += 2) { v[j] -= spl(m); v[j + 1] -= spl(m); q = fma2(v[j], v[j], q); q1 = fma2(v[j + 1], v[j + 1], q1); }
      q += q1;
      const float var = hsum32(q.x + q.y) * (1.f / 512.f);
      const float mb = rb(m), r1 = __builtin_amdgcn_rcpf(rb(__builtin_amdgcn_sqrtf(rb(rb(var) + 1e-5f))));
      const float dm = (m - mb) * r1, r2 = __builtin_amdgcn_rcpf(rb(__builtin_amdgcn_sqrtf(rb(rb(var * r1 * r1) + 1e-5f))));
      const f32x2 rr = spl(r1 * r2), nb = spl((dm - rb(dm)) * r2);
      const u32 row = 2 * w + hh, ha = HIM + row * 1024 + ((l31 ^ ((row & 3) << 2)) << 4);
      sts128(ha, v4u{pk2(fma2(v[0], rr, nb)), pk2(fma2(v[1], rr, nb)), pk2(fma2(v[2], rr, nb)), pk2(fma2(v[3], rr, nb))});
      sts128(ha + 512, v4u{pk2(fma2(v[4], rr, nb)), pk2(fma2(v[5], rr, nb)), pk2(fma2(v[6], rr, nb)), pk2(fma2(v[7], rr, nb))});
      asm volatile("s_waitcnt vmcnt(2)" : "+v"(ep)::"memory");
      tlo = rfl(ep.x) + 1u; thi = rfl(ep.y);
      stat = v4u{pk(-mb, 0.f), tlo, __float_as_uint(r1), tlo};
    }
    asm volatile("s_waitcnt lgkmcnt(0)" ::: "memory");
    *(volatile LAS u32*)(size_t)(SYN + w * 4) = 1u;
    all8(SYN);
#pragma unroll
    for (int i = 0; i < 2; ++i) {
      const u32 d32 = 2 * w + i;
      const u32 a = HIM + (8 * hh + q4) * 1024 + (((d32 * 4 + 2 * blk + (p4 >> 1)) ^ (q4 << 2)) << 4) + (p4 & 1) * 8;
      st128_sc1(trfrag(a, a + 4096), rsWb, WS_H + (d32 * 128 + bid) * 1024 + lane16);
    }
    asm volatile("s_waitcnt vmcnt(0)" : "+v"(bias0), "+v"(bias1)::"memory");
    publish(rsWb, F1 + (w * 128 + bid) * 8, tlo, thi, lane);
    *(volatile LAS u32*)(size_t)(SYN + 32 + w * 4) = 1u;
    if (l31 == 0) st128_sc1(stat, rsWb, WS_STATS + (bid * 16 + 2 * w + hh) * 16);
    v4u s0, s1;
    sum_selectors(lane, s0, s1);
    f32x2 bv[4] = {unp(bias0.x), unp(bias0.y), unp(bias1.x), unp(bias1.y)};
    u32 ra = RED + ((hh * 2 + fd) * 6 + gs) * 1024 + l31 * 16, uo = WS_U + ((d64 * 2 + fd) * 86 + g0 * 2 + gs) * 1024 + lane16;
    PIN(bv[0]); PIN(bv[1]); PIN(bv[2]); PIN(bv[3]); PIN(ra); PIN(uo);
    f32x16 acc[3] = {zero16(), zero16(), zero16()};
    u32 wa = kq * 6144 + (8 * hh + q4) * 192 + blk * 32 + p4 * 8;
    PIN(acc[0]); PIN(acc[1]); PIN(acc[2]); PIN(wa);
    if (dh == 0) {
      __builtin_amdgcn_s_sleep(2);
      spoll32(ws + WS_FLAGS + xcd * FL_REP + F1 + d64 * 1024 + kq * 256, tag64(tlo, thi));
      *(volatile LAS u32*)(size_t)(SYN + 68 + kq * 16) = 1u;
    } else {
#pragma unroll 1
      for (u32 spin = 0; spin < (SPIN_CAP << 4) && !go_seen(SYN + 68 + kq * 16, 1u); ++spin) __builtin_amdgcn_s_sleep(1);
    }
    const v4u rsH = mkrs(ws + WS_H + ((d64 * 2 + dh) * 128 + kq * 32) * 1024, 32768u);
    v4u hf[8];
#pragma unroll
    for (int i = 0; i < 8; ++i) ld128_sc1_new(hf[i], lane16, rsH, ((brot + i) & 31) * 1024);
    LBAR();
    v4u wf[2][3];
    auto rd = [&](int buf, u32 a) {
#pragma unroll
      for (int g = 0; g < 3; ++g) wf[buf][g] = trfrag(a + g * 64, a + 768 + g * 64);
    };
    rd(0, wa);
    u32 sb = 0, so = brot * 1024;
    auto round = [&](auto last) {
      constexpr bool LAST = decltype(last)::value;
#pragma unroll
      for (int i = 0; i < 8; ++i) {
        const int c = i & 1, n = c ^ 1;
        if (LAST) asm volatile("s_waitcnt vmcnt(%1)" : "+v"(hf[i]) : "n"(7 - i) : "memory");
        else asm volatile("s_waitcnt vmcnt(7)" : "+v"(hf[i])::"memory");
        SCHED();
        acc[0] = mfma(wf[c][0], hf[i], acc[0]); SCHED();
        if (c) { BAR(); sb = sb == 5 * 24576 ? 0u : sb + 24576; }
        const u32 a = sb + wa + (c ? 0 : 3072);
        wf[n][0] = trfrag(a, a + 768); wf[n][1] = trfrag(a + 64, a + 768 + 64); SCHED();
        acc[1] = mfma(wf[c][1], hf[i], acc[1]); SCHED();
        wf[n][2] = trfrag(a + 128, a + 768 + 128); SCHED();
        acc[2] = mfma(wf[c][2], hf[i], acc[2]);
        if (!LAST) ld128_sc1(hf[i], lane16, rsH, (so + i * 1024) & 32767);
        SCHED();
      }
    };
#pragma unroll 1
    for (u32 r = 0; r < 3; ++r) { so += 8192; round(BoolC<false>{}); }
    round(BoolC<true>{});
#pragma unroll
    for (int g = 0; g < 3; ++g) {
      sts128(RED + (w * 6 + 2 * g) * 1024 + lane16, lo8(acc[g]));
      sts128(RED + (w * 6 + 2 * g + 1) * 1024 + lane16, hi8(acc[g]));
    }
    BAR();
    {
      const v4u o = gelu_frag(ra, s0, s1, bv);
      if ((gs >> 1) < gown) st128_sc1(o, rsWb, uo);
    }
    DRAIN_BAR();
    if (w == 0) publish(rsWb, F4 + (d64 * 16 + ft) * 8, tlo, thi, lane);
  }

  const u32 b = opq(blockIdx.x);
  GEO_BASE(b); GEO_F();
  const bool isWO = kq == 3;
  f32x16 acc[2] = {zero16(), zero16()};
  u32 fa = kq * 4096 + (4 * hh + q4) * 128 + ((q4 >> 1) << 6) + blk * 32 + p4 * 8;
  const v4u rsA = isWO ? mkrs(ws + WS_O + (tt * 2 + dh) * 32768, 32768) : mkrs(ws + WS_U + ((dt * 2 + dh) * 86 + kq * 29) * 1024, kq == 2 ? 28 * 1024 : 29 * 1024);
  PIN(acc[0]); PIN(acc[1]); PIN(fa);
  asm volatile("" ::"s"(rsA));
  LBAR();
  v4u rf[8][2];
#pragma unroll
  for (int i = 0; i < 8; ++i) {
    const u32 a = fa + (i >> 1) * 16384 + (i & 1) * 2048;
    rf[i][0] = trfrag(a, a + 1024);
    rf[i][1] = trfrag(a ^ 64, (a ^ 64) + 1024);
  }
  asm volatile("s_waitcnt lgkmcnt(0)\n\ts_barrier"
               : "+v"(rf[0][0]), "+v"(rf[0][1]), "+v"(rf[1][0]), "+v"(rf[1][1]), "+v"(rf[2][0]), "+v"(rf[2][1]), "+v"(rf[3][0]), "+v"(rf[3][1]),
                 "+v"(rf[4][0]), "+v"(rf[4][1]), "+v"(rf[5][0]), "+v"(rf[5][1]), "+v"(rf[6][0]), "+v"(rf[6][1]), "+v"(rf[7][0]), "+v"(rf[7][1])::"memory");
  if (w == 0) {
    spoll24(ws + WS_FLAGS + xcd * FL_REP + F4 + dt * 128, ws + WS_FLAGS + xcd * FL_REP + F3 + (tt >> 1) * 64, tag64(tlo, thi));
    *(volatile LAS u32*)(size_t)GO2 = MAGIC2;
    if (b == 0 && lane == 0) st64_sc1(v2u{tlo, thi}, rsWb, WS_EPOCH);
  }
  LBAR();
  v4u af[8];
#pragma unroll
  for (int i = 0; i < 8; ++i) ld128_sc1_new(af[i], lane16, rsA, ((frot + i) & 31) * 1024);
  v4u wf[2][2];
  auto rd = [&](int buf, u32 a) {
    wf[buf][0] = trfrag(a, a + 1024);
    wf[buf][1] = trfrag(a ^ 64, (a ^ 64) + 1024);
  };
  u32 sb = 4 * 16384, so = frot * 1024 + 8192;
#pragma unroll
  for (int i = 0; i < 8; ++i) {
    asm volatile("s_waitcnt vmcnt(7)" : "+v"(af[i])::"memory");
    SCHED();
    acc[0] = mfma(af[i], rf[i][0], acc[0]); acc[1] = mfma(af[i], rf[i][1], acc[1]);
    ld128_sc1(af[i], lane16, rsA, (so + i * 1024) & 32767);
    if (i == 6) rd(0, sb + fa);
    SCHED();
  }
  auto round = [&](auto last) {
    constexpr bool LAST = decltype(last)::value;
#pragma unroll
    for (int i = 0; i < 8; ++i) {
      const int c = i & 1, n = c ^ 1;
      if (LAST) asm volatile("s_waitcnt vmcnt(%1)" : "+v"(af[i]) : "n"(9 - i) : "memory");
      else asm volatile("s_waitcnt vmcnt(7)" : "+v"(af[i])::"memory");
      SCHED();
      acc[0] = mfma(af[i], wf[c][0], acc[0]); SCHED();
      if (c) { BAR(); sb = sb == 8 * 16384 ? 0u : sb + 16384; }
      rd(n, sb + fa + (c ? 0 : 2048)); SCHED();
      acc[1] = mfma(af[i], wf[c][1], acc[1]);
      if (!LAST) ld128_sc1(af[i], lane16, rsA, (so + i * 1024) & 32767);
      SCHED();
    }
  };
#pragma unroll 1
  for (u32 r = 0; r < 2; ++r) { so += 8192; round(BoolC<false>{}); }
  v4u xres, S0, S1, S2, S3;
  u32 b2r, eoff, pw, ta;
  {
    u32 ln = lane;
    PIN(ln);
    const u32 tk = tt * 64 + ((w >> 1) & 1) * 32 + (ln & 31), h1 = ln >> 5, r4 = (ln & 15) >> 2;
    eoff = (tk * 512 + dt * 64 + (w >> 2) * 32 + (w & 1) * 16 + h1 * 8) * 2;
    ld128_new(xres, eoff, rsX, 0u);
    asm volatile("buffer_load_ushort %0, %1, %2, 0 offen" : "=v"(b2r) : "v"(tk * 2), "s"(mkrs(gb2, 4096)) : "memory");
    const u32 sv = (ln & 1) ? 0x3f800000u : 0x00003f80u, m = (ln >> 1) & 3, g = ln >> 3;
    const v4u e = {m == 0 ? sv : 0u, m == 1 ? sv : 0u, m == 2 ? sv : 0u, m == 3 ? sv : 0u}, z = {0u, 0u, 0u, 0u};
    S0 = (g & 3) == 0 ? e : z; S1 = (g & 3) == 1 ? e : z; S2 = g == 0 ? e : z; S3 = g == 1 ? e : z;
    pw = isWO ? 24576 + (ln & 31) * 128 + (((dh * 4) ^ (ln & 7)) << 4) + h1 * 8 : kq * 8192 + dh * 4096 + ln * 16;
    ta = 24576 + ((w >> 2) * 32 + (w & 1) * 16 + 8 * h1 + r4) * 128 + (((((w >> 1) & 1) * 4 + ((ln >> 4) & 1) * 2 + ((ln & 3) >> 1)) ^ r4) << 4) + (ln & 1) * 8;
    PIN(S0); PIN(S1); PIN(S2); PIN(S3); PIN(pw); PIN(ta);
  }
  round(BoolC<true>{});
  {
    const v4u y0 = lo8(acc[0]), y1 = hi8(acc[0]), y2 = lo8(acc[1]), y3 = hi8(acc[1]);
    if (isWO) {
      sts64(pw, v2u{y0.x, y0.y}); sts64(pw ^ 16, v2u{y0.z, y0.w}); sts64(pw ^ 32, v2u{y1.x, y1.y}); sts64(pw ^ 48, v2u{y1.z, y1.w});
      sts64(pw + 4096, v2u{y2.x, y2.y}); sts64((pw ^ 16) + 4096, v2u{y2.z, y2.w}); sts64((pw ^ 32) + 4096, v2u{y3.x, y3.y}); sts64((pw ^ 48) + 4096, v2u{y3.z, y3.w});
    } else {
      sts128(pw, y0); sts128(pw + 1024, y1); sts128(pw + 2048, y2); sts128(pw + 3072, y3);
    }
  }
  asm volatile("s_waitcnt vmcnt(0) lgkmcnt(0)\n\ts_barrier" : "+v"(xres), "+v"(b2r)::"memory");
  {
    const u32 a = (lane16 >> 9) * 8192 + w * 1024 + (lane16 & 511), c = 16384 + w * 1024 + (lane16 & 511);
    f32x16 t = mfma(S0, lds128(a), zero16());
    t = mfma(S1, lds128(a + 512), t);
    t = mfma(S2, lds128(c), t);
    t = mfma(S3, lds128(c + 512), t);
    const v4u sv = trfrag(ta, (ta ^ 64) + 512);
    const float b2v = blo(b2r);
    v4u o;
#pragma unroll
    for (int j = 0; j < 4; ++j) {
      f32x2 yy = f32x2{t[2 * j], t[2 * j + 1]} + spl(b2v), e = yy * spl(-LOG2E);
      f32x2 d = f32x2{__builtin_amdgcn_exp2f(e.x), __builtin_amdgcn_exp2f(e.y)} + spl(1.f);
      f32x2 g = yy * f32x2{__builtin_amdgcn_rcpf(d.x), __builtin_amdgcn_rcpf(d.y)};
      u32 xd = xres[j], sd = sv[j];
      o[j] = pk2(fma2(unp(sd), g, unp(xd)));
    }
    *(v4u*)((char*)gout + eoff) = o;
  }
}

extern "C" void kernel_launch(void* const* d_in, const int* in_sizes, int n_in, void* d_out, int out_size, void* d_ws, size_t ws_size,
                              hipStream_t stream) {
  (void)in_sizes; (void)n_in; (void)out_size; (void)ws_size;
  (void)hipFuncSetAttribute((const void*)fused_kernel, hipFuncAttributeMaxDynamicSharedMemorySize, LDS_BYTES);
  fused_kernel<<<256, 768, LDS_BYTES, stream>>>((const u16*)d_in[0], (const u16*)d_in[1], (const u16*)d_in[2], (const u16*)d_in[3],
                                                (const u16*)d_in[5], (const u16*)d_in[6], (const u16*)d_in[7], (const u16*)d_in[8],
                                                (u16*)d_out, (char*)d_ws);
}
```
